```python
import torch

def _softmax(x, axis=-1):
    return torch.softmax(x, dim=axis)
def _gelu(x):
    return torch.nn.functional.gelu(x, approximate="tanh")
def _silu(x):
    return torch.nn.functional.silu(x)
def _sigmoid(x):
    return torch.sigmoid(x)
def _layernorm(x, axis=-1, eps=1e-5):
    m = x.mean(dim=axis, keepdim=True)
    v = x.var(dim=axis, keepdim=True, unbiased=False)
    return (x - m) / torch.sqrt(v + eps)
def _scatter_add(tgt, idx, src, axis=0):
    return tgt.clone().index_add_(axis, idx, src)
def _segment_sum(src, seg, n):
    out = torch.zeros((n,) + src.shape[1:], dtype=src.dtype)
    return out.index_add_(0, seg, src)
def _pad(x, pw):
    flat = tuple(v for pair in reversed(pw) for v in pair)
    return torch.nn.functional.pad(x, flat)

kernel_name = 'standalone__mha_block_cached__near_ridge'

def setup_inputs():

    _w = (lambda a: (lambda t: t.to(torch.bfloat16) if t.is_floating_point() else t)(torch.as_tensor(a)))
    B, Sq, D = 8, 144, 576
    H, Sk, Dh = 32, 512, 128
    return {
        "x": _w((lambda *s: torch.randn(*s, dtype=torch.bfloat16) * 0.8)(B, Sq, D)),
        "k_cache": _w((lambda *s: torch.randn(*s, dtype=torch.bfloat16) * 0.8)(B, H, Sk, Dh)),
        "v_cache": _w((lambda *s: torch.randn(*s, dtype=torch.bfloat16) * 0.05)(B, H, Sk, Dh)),
        "wq": _w((lambda *s: torch.randn(*s, dtype=torch.bfloat16) * 0.8)(D, H * Dh) * (D ** -0.5)),
        "wo": _w((lambda *s: torch.randn(*s, dtype=torch.bfloat16) * 0.05)(H * Dh, D) * ((H * Dh) ** -0.5)),
    }

def reference(x, k_cache, v_cache, wq, wo):
    B, Sq, D = x.shape
    H, Dh = 32, 128
    q = (lambda x, *d: x.permute(*d))((x @ wq).reshape(B, Sq, H, Dh), 0, 2, 1, 3)
    scores = torch.einsum("bhqd,bhkd->bhqk", q, k_cache) * (Dh ** -0.5)
    p = _softmax(scores, axis=-1)
    o = torch.einsum("bhqk,bhkd->bhqd", p, v_cache)
    return (lambda x, *d: x.permute(*d))(o, 0, 2, 1, 3).reshape(B, Sq, H * Dh) @ wo
```

```cpp
#include <hip/hip_runtime.h>
#include <hip/hip_bf16.h>

constexpr int B=8, SQ=144, D=576, H=32, SK=512, DH=128;

__device__ __forceinline__ float bf2f(unsigned short v){ return __uint_as_float(((unsigned)v)<<16); }
__device__ __forceinline__ unsigned short f2bf(float f){ unsigned u=__float_as_uint(f); u += 0x7fffu + ((u>>16)&1u); return (unsigned short)(u>>16); }

__global__ void qproj(const unsigned short* x, const unsigned short* wq, unsigned short* q){
  int n = blockIdx.x*256+threadIdx.x;
  int m = blockIdx.y;
  float acc=0.f;
  for(int k=0;k<D;k++) acc += bf2f(x[m*D+k])*bf2f(wq[(size_t)k*4096+n]);
  q[(size_t)m*4096+n]=f2bf(acc);
}
__global__ void attn(const unsigned short* q, const unsigned short* kc, const unsigned short* vc, unsigned short* o){
  int qi = blockIdx.x % SQ; int bh = blockIdx.x / SQ; int b = bh / H, h = bh % H;
  __shared__ float s[SK]; __shared__ float qv[DH]; __shared__ float red[128];
  int t=threadIdx.x;
  qv[t]=bf2f(q[((size_t)(b*SQ+qi))*4096 + h*DH + t]);
  __syncthreads();
  const unsigned short* K = kc + (size_t)bh*SK*DH; const unsigned short* V = vc + (size_t)bh*SK*DH;
  float mx=-1e30f;
  for(int k=t;k<SK;k+=128){ float a=0; for(int d=0;d<DH;d++) a+=qv[d]*bf2f(K[k*DH+d]); a*=0.08838834764831845f; s[k]=a; mx=fmaxf(mx,a);}
  red[t]=mx; __syncthreads();
  for(int o2=64;o2>0;o2>>=1){ if(t<o2) red[t]=fmaxf(red[t],red[t+o2]); __syncthreads(); }
  mx=red[0]; __syncthreads();
  float sum=0;
  for(int k=t;k<SK;k+=128){ float e=__expf(s[k]-mx); s[k]=e; sum+=e; }
  red[t]=sum; __syncthreads();
  for(int o2=64;o2>0;o2>>=1){ if(t<o2) red[t]+=red[t+o2]; __syncthreads(); }
  sum=red[0];
  float a=0; for(int k=0;k<SK;k++) a+=s[k]*bf2f(V[k*DH+t]);
  o[((size_t)(b*SQ+qi))*4096 + h*DH + t]=f2bf(a/sum);
}
__global__ void oproj(const unsigned short* o, const unsigned short* wo, unsigned short* out){
  int n = blockIdx.x*64+threadIdx.x;
  int m = blockIdx.y;
  float acc=0.f;
  for(int k=0;k<4096;k++) acc += bf2f(o[(size_t)m*4096+k])*bf2f(wo[(size_t)k*D+n]);
  out[(size_t)m*D+n]=f2bf(acc);
}
extern "C" void kernel_launch(void* const* d_in, const int* in_sizes, int n_in,
                              void* d_out, int out_size, void* d_ws, size_t ws_size,
                              hipStream_t stream) {
  const unsigned short* x=(const unsigned short*)d_in[0];
  const unsigned short* kc=(const unsigned short*)d_in[1];
  const unsigned short* vc=(const unsigned short*)d_in[2];
  const unsigned short* wq=(const unsigned short*)d_in[3];
  const unsigned short* wo=(const unsigned short*)d_in[4];
  unsigned short* q=(unsigned short*)d_ws; unsigned short* o=q+(size_t)1152*4096;
  qproj<<<dim3(16,1152),256,0,stream>>>(x,wq,q);
  attn<<<B*H*SQ,128,0,stream>>>(q,kc,vc,o);
  oproj<<<dim3(9,1152),64,0,stream>>>(o,wo,(unsigned short*)d_out);
}
```

```cpp
#include <hip/hip_runtime.h>
#include <hip/hip_bf16.h>

#define LAS __attribute__((address_space(3)))
#define DI __device__ __forceinline__
typedef short bf16x8 __attribute__((ext_vector_type(8)));
typedef short s16x4 __attribute__((ext_vector_type(4)));
typedef float f32x4 __attribute__((ext_vector_type(4)));
typedef float f32x2 __attribute__((ext_vector_type(2)));
typedef __bf16 bf16x2 __attribute__((ext_vector_type(2)));
typedef unsigned u32x4 __attribute__((ext_vector_type(4)));
typedef unsigned u32x2 __attribute__((ext_vector_type(2)));

constexpr int SQ = 144, DM = 576, NH = 32, SK = 512, DH = 128;
constexpr unsigned SLOT = 18432, MISC = 8 * SLOT, LDS_BYTES = MISC + 16384;
constexpr float SL2E = 0.08838834764831845f * 1.4426950408889634f;

DI unsigned rfl(unsigned x) { return __builtin_amdgcn_readfirstlane(x); }
DI unsigned cvtpk(float lo, float hi) { f32x2 v = {lo, hi}; bf16x2 b = __builtin_convertvector(v, bf16x2); return __builtin_bit_cast(unsigned, b); }
DI bf16x8 lds128(unsigned a) { return *(const LAS bf16x8*)(size_t)a; }
DI u32x2 lds64(unsigned a) { return *(const LAS u32x2*)(size_t)a; }
DI s16x4 ldstr(unsigned a) { return __builtin_amdgcn_ds_read_tr16_b64_v4i16((LAS s16x4*)(size_t)a); }
DI bf16x8 cat(s16x4 lo, s16x4 hi) { return __builtin_shufflevector(lo, hi, 0, 1, 2, 3, 4, 5, 6, 7); }
DI f32x4 mfma(bf16x8 a, bf16x8 b, f32x4 c) { return __builtin_amdgcn_mfma_f32_16x16x32_bf16(a, b, c, 0, 0, 0); }

DI void dma16(unsigned ldsdst, unsigned voff, __amdgpu_buffer_rsrc_t srd, unsigned soff) {
  asm volatile("s_mov_b32 m0, %0\n\ts_nop 0\n\tbuffer_load_dwordx4 %1, %2, %3 offen lds" :: "s"(ldsdst), "v"(voff), "s"(srd), "s"(soff) : "memory");
}
DI void dma16nt(unsigned ldsdst, unsigned voff, __amdgpu_buffer_rsrc_t srd, unsigned soff) {
  asm volatile("s_mov_b32 m0, %0\n\ts_nop 0\n\tbuffer_load_dwordx4 %1, %2, %3 offen nt lds" :: "s"(ldsdst), "v"(voff), "s"(srd), "s"(soff) : "memory");
}
DI void dma4(unsigned ldsdst, unsigned voff, __amdgpu_buffer_rsrc_t srd, unsigned soff) {
  asm volatile("s_mov_b32 m0, %0\n\ts_nop 0\n\tbuffer_load_dword %1, %2, %3 offen lds" :: "s"(ldsdst), "v"(voff), "s"(srd), "s"(soff) : "memory");
}
template <int N> DI void wait_bar() { asm volatile("s_waitcnt vmcnt(%0) lgkmcnt(0)\n\ts_barrier" :: "i"(N) : "memory"); }
DI void lds_bar() { asm volatile("s_waitcnt lgkmcnt(0)\n\ts_barrier" ::: "memory"); }

#define KV_NT 1
DI void dmakv(unsigned ldsdst, unsigned voff, __amdgpu_buffer_rsrc_t srd, unsigned soff) {
  if (KV_NT) dma16nt(ldsdst, voff, srd, soff); else dma16(ldsdst, voff, srd, soff);
}

__global__ __launch_bounds__(512, 2) void k1_attn(const unsigned short* __restrict__ x, const unsigned short* __restrict__ kc,
                                                  const unsigned short* __restrict__ vc, const unsigned short* __restrict__ wq,
                                                  unsigned short* __restrict__ obuf) {
  extern __shared__ __attribute__((aligned(16))) char lds[];
  const unsigned lb = (unsigned)(size_t)lds;
  const unsigned tid = threadIdx.x, l = tid & 63u, c = l & 15u, G = l >> 4, q4 = (l >> 2) & 3u, p = l & 3u;
  const unsigned w = rfl(tid >> 6);
  const unsigned blk = blockIdx.x, g = blk & 7u, r_ = blk >> 3, j = r_ & 3u, b = r_ >> 2, h = 4u * g + j;

  const __amdgpu_buffer_rsrc_t srdX = __builtin_amdgcn_make_buffer_rsrc((void*)(x + (size_t)b * SQ * DM), (short)0, SQ * DM * 2, 0x00020000);
  const __amdgpu_buffer_rsrc_t srdW = __builtin_amdgcn_make_buffer_rsrc((void*)(wq + (size_t)h * DH), (short)0, (DM * 4096 - h * DH) * 2, 0x00020000);
  const __amdgpu_buffer_rsrc_t srdK = __builtin_amdgcn_make_buffer_rsrc((void*)(kc + (size_t)(b * NH + h) * SK * DH), (short)0, SK * DH * 2, 0x00020000);
  const __amdgpu_buffer_rsrc_t srdV = __builtin_amdgcn_make_buffer_rsrc((void*)(vc + (size_t)(b * NH + h) * SK * DH), (short)0, SK * DH * 2, 0x00020000);

  const unsigned vx0 = (16u * w + (l >> 3)) * 1152u + (((l & 7u) ^ (G & 7u)) << 4);
  const unsigned vx1 = (16u * w + 8u + (l >> 3)) * 1152u + (((l & 7u) ^ ((4u + G) & 7u)) << 4);
  const unsigned vx9 = (128u + 2u * w + (l >> 5)) * 1152u + (((((l & 31u) >> 2) ^ w) & 7u) << 4) + ((l & 3u) << 2);
  const unsigned vw = (8u * w + G) * 8192u + ((((c >> 1) ^ (G | ((w & 1u) << 2)))) << 5) + ((c & 1u) << 4);
  const unsigned vk0 = (8u * w + G) * 256u + ((c ^ (8u * (w & 1u) + G)) << 4);
  const unsigned vk1 = (8u * w + 4u + G) * 256u + ((c ^ (8u * (w & 1u) + 4u + G)) << 4);
  const unsigned vv0 = (8u * w + G) * 256u + (((c >> 1) ^ G) << 5) + ((c & 1u) << 4);
  const unsigned vv1 = (8u * w + 4u + G) * 256u + (((c >> 1) ^ (4u + G)) << 5) + ((c & 1u) << 4);

  auto issueX = [&](int i) { const unsigned s = lb + ((2 * i) & 7) * SLOT;
    dma16(s + w * 2048u, vx0, srdX, i * 128); dma16(s + w * 2048u + 1024u, vx1, srdX, i * 128); dma4(s + 16384u + w * 256u, vx9, srdX, i * 128); };
  auto issueW = [&](int i) { const unsigned s = lb + ((2 * i + 1) & 7) * SLOT + w * 2048u;
    dma16(s, vw, srdW, (64 * i) * 8192); dma16(s + 1024u, vw, srdW, (64 * i + 4) * 8192); };
  auto issueK = [&](int t) { const unsigned s = lb + ((2 + t) & 7) * SLOT + w * 2048u;
    dmakv(s, vk0, srdK, t * 16384); dmakv(s + 1024u, vk1, srdK, t * 16384); };
  auto issueV = [&](int t) { const unsigned s = lb + ((2 + t) & 7) * SLOT + w * 2048u;
    dmakv(s, vv0, srdV, t * 16384); dmakv(s + 1024u, vv1, srdV, t * 16384); };

  asm volatile("s_nop 4");
  issueX(0); issueW(0); issueX(1); issueW(1); issueX(2); issueW(2); issueX(3); issueW(3);

  const unsigned axo = (16u * w + c) * 128u + ((G ^ (c >> 1)) << 4);
  const unsigned ax9 = (128u + c) * 128u + ((G ^ (c >> 1)) << 4);
  const unsigned awb = (8u * G + q4) * 256u + ((w ^ (q4 | ((G & 1u) << 2))) << 5) + 8u * p;
  f32x4 qa[8], qa9 = {0.f, 0.f, 0.f, 0.f};
#pragma unroll
  for (int t = 0; t < 8; ++t) qa[t] = (f32x4){0.f, 0.f, 0.f, 0.f};

  auto qstep = [&](int i) {
    const unsigned sx = lb + ((2 * i) & 7) * SLOT, sw = lb + ((2 * i + 1) & 7) * SLOT;
#pragma unroll
    for (int ks = 0; ks < 2; ++ks) {
      const bf16x8 bo = lds128(sx + (axo ^ (ks << 6))), b9 = lds128(sx + (ax9 ^ (ks << 6)));
#pragma unroll
      for (int t = 0; t < 8; ++t) {
        const unsigned a = sw + ks * 8192 + (awb ^ (t << 5));
        const bf16x8 af = cat(ldstr(a), ldstr(a + 1024));
        qa[t] = mfma(af, bo, qa[t]);
        if (t == 0) qa9 = mfma(af, b9, qa9);
      }
    }
  };
  wait_bar<15>(); qstep(0);
  wait_bar<10>(); issueX(4); issueW(4); qstep(1);
  wait_bar<10>(); issueX(5); issueW(5); qstep(2);
  wait_bar<10>(); issueX(6); issueW(6); qstep(3);
  wait_bar<10>(); issueX(7); issueW(7); qstep(4);
  wait_bar<10>(); issueX(8); issueW(8); qstep(5);
  wait_bar<10>(); issueK(0); issueK(1); qstep(6);
  wait_bar<9>();  issueK(2); issueK(3); qstep(7);
  wait_bar<8>();  issueK(4); issueK(5); qstep(8);

  bf16x8 qf[4];
#pragma unroll
  for (int s = 0; s < 4; ++s) {
    unsigned a0 = cvtpk(qa[2 * s][0], qa[2 * s][1]), a1 = cvtpk(qa[2 * s][2], qa[2 * s][3]);
    unsigned b0 = cvtpk(qa[2 * s + 1][0], qa[2 * s + 1][1]), b1 = cvtpk(qa[2 * s + 1][2], qa[2 * s + 1][3]);
    auto r0 = __builtin_amdgcn_permlane32_swap(a0, b0, false, false); auto r1 = __builtin_amdgcn_permlane32_swap(a1, b1, false, false);
    auto s0 = __builtin_amdgcn_permlane16_swap(r0[0], r0[1], false, false); auto s1 = __builtin_amdgcn_permlane16_swap(r1[0], r1[1], false, false);
    u32x4 f = {s0[0], s1[0], s0[1], s1[1]};
    qf[s] = __builtin_bit_cast(bf16x8, f);
  }
  { u32x2 v = {cvtpk(qa9[0], qa9[1]), cvtpk(qa9[2], qa9[3])};
    *(LAS u32x2*)(size_t)(lb + MISC + c * 256u + ((w * 32u + G * 8u) ^ (c << 4))) = v; }

  const unsigned akb = c * 256u + (((G ^ (2u * w) ^ c) & 15u) << 4);
  f32x4 S[8][4], S9[4];
  bf16x8 q9f[4];
  float mx = -3.0e38f, mx9 = -3.0e38f;
  auto kstep = [&](int t) {
    const unsigned sk = lb + ((2 + t) & 7) * SLOT;
    const bool duty = ((unsigned)(t & 1) == (w >> 2));
#pragma unroll
    for (int kt = 0; kt < 4; ++kt) {
      const unsigned rowb = sk + ((kt ^ (w & 3u)) << 12);
      f32x4 acc = {0.f, 0.f, 0.f, 0.f}, acc9 = {0.f, 0.f, 0.f, 0.f};
      bf16x8 kf[4];
#pragma unroll
      for (int s = 0; s < 4; ++s) kf[s] = lds128(rowb + (akb ^ (s << 6)));
#pragma unroll
      for (int s = 0; s < 4; ++s) acc = mfma(kf[s], qf[s], acc);
      S[t][kt] = acc;
      mx = fmaxf(mx, fmaxf(fmaxf(acc[0], acc[1]), fmaxf(acc[2], acc[3])));
      if (kt == 0) { if (duty) {
#pragma unroll
        for (int s = 0; s < 4; ++s) acc9 = mfma(kf[s], q9f[s], acc9);
        S9[t >> 1] = acc9;
        mx9 = fmaxf(mx9, fmaxf(fmaxf(acc9[0], acc9[1]), fmaxf(acc9[2], acc9[3]))); } }
    }
  };
  wait_bar<10>(); issueK(6); issueK(7);
#pragma unroll
  for (int s = 0; s < 4; ++s) q9f[s] = lds128(lb + MISC + c * 256u + ((((4u * s + G) ^ (2u * w) ^ c) & 15u) << 4));
  kstep(0);
  wait_bar<12>(); issueV(0); kstep(1);
  wait_bar<12>(); issueV(1); kstep(2);
  wait_bar<12>(); issueV(2); kstep(3);
  wait_bar<12>(); issueV(3); kstep(4);
  wait_bar<12>(); issueV(4); kstep(5);
  wait_bar<12>(); issueV(5); kstep(6);
  wait_bar<12>(); issueV(6); kstep(7);

  mx = fmaxf(mx, __shfl_xor(mx, 16)); mx = fmaxf(mx, __shfl_xor(mx, 32));
  mx9 = fmaxf(mx9, __shfl_xor(mx9, 16)); mx9 = fmaxf(mx9, __shfl_xor(mx9, 32));
  const unsigned XCH = lb + 16384u;
  if (G == 0) *(LAS float*)(size_t)(XCH + w * 64u + c * 4u) = mx9;
  lds_bar();
  issueV(7);
  {
    float m = -3.0e38f;
#pragma unroll
    for (int i = 0; i < 8; ++i) m = fmaxf(m, *(const LAS float*)(size_t)(XCH + i * 64u + c * 4u));
    mx9 = m;
  }
  float l9 = 0.f;
  {
    const float nm9 = -mx9 * SL2E;
#pragma unroll
    for (int tp = 0; tp < 4; ++tp) {
      float e0 = __builtin_amdgcn_exp2f(fmaf(S9[tp][0], SL2E, nm9)), e1 = __builtin_amdgcn_exp2f(fmaf(S9[tp][1], SL2E, nm9));
      float e2 = __builtin_amdgcn_exp2f(fmaf(S9[tp][2], SL2E, nm9)), e3 = __builtin_amdgcn_exp2f(fmaf(S9[tp][3], SL2E, nm9));
      l9 += (e0 + e1) + (e2 + e3);
      u32x2 v = {cvtpk(e0, e1), cvtpk(e2, e3)};
      const unsigned ktg = 4u * (2u * tp + (w >> 2)) + (w & 3u);
      *(LAS u32x2*)(size_t)(lb + MISC + ktg * 512u + l * 8u) = v;
    }
    l9 += __shfl_xor(l9, 16); l9 += __shfl_xor(l9, 32);
    if (G == 0) *(LAS float*)(size_t)(XCH + 512u + w * 64u + c * 4u) = l9;
  }
  bf16x8 P[8][2];
  float ls = 0.f;
  {
    const float nm = -mx * SL2E;
#pragma unroll
    for (int t = 0; t < 8; ++t)
#pragma unroll
      for (int pp = 0; pp < 2; ++pp) {
        unsigned d[4];
#pragma unroll
        for (int hb = 0; hb < 2; ++hb) {
          const f32x4 s = S[t][2 * pp + hb];
          float e0 = __builtin_amdgcn_exp2f(fmaf(s[0], SL2E, nm)), e1 = __builtin_amdgcn_exp2f(fmaf(s[1], SL2E, nm));
          float e2 = __builtin_amdgcn_exp2f(fmaf(s[2], SL2E, nm)), e3 = __builtin_amdgcn_exp2f(fmaf(s[3], SL2E, nm));
          ls += (e0 + e1) + (e2 + e3);
          d[2 * hb] = cvtpk(e0, e1); d[2 * hb + 1] = cvtpk(e2, e3);
        }
        u32x4 f = {d[0], d[1], d[2], d[3]};
        P[t][pp] = __builtin_bit_cast(bf16x8, f);
      }
    ls += __shfl_xor(ls, 16); ls += __shfl_xor(ls, 32);
  }

  const unsigned avb = (4u * G + q4) * 256u + ((w ^ (4u * (G & 1u) + q4)) << 5) + 8u * p;
  f32x4 O[8], O9 = {0.f, 0.f, 0.f, 0.f};
#pragma unroll
  for (int u = 0; u < 8; ++u) O[u] = (f32x4){0.f, 0.f, 0.f, 0.f};
  auto vstep = [&](int t) {
    const unsigned sv = lb + ((2 + t) & 7) * SLOT;
#pragma unroll
    for (int pp = 0; pp < 2; ++pp) {
      const unsigned k0 = ((2 * pp) ^ (w & 3u)), k1 = ((2 * pp + 1) ^ (w & 3u));
      const u32x2 pa = lds64(lb + MISC + (4u * t + k0) * 512u + l * 8u), pb = lds64(lb + MISC + (4u * t + k1) * 512u + l * 8u);
      const u32x4 p9u = {pa[0], pa[1], pb[0], pb[1]};
      const bf16x8 p9 = __builtin_bit_cast(bf16x8, p9u);
#pragma unroll
      for (int u = 0; u < 8; ++u) {
        const unsigned a = (avb ^ (u << 5));
        const bf16x8 af = cat(ldstr(sv + (k0 << 12) + a), ldstr(sv + (k1 << 12) + a));
        O[u] = mfma(af, P[t][pp], O[u]);
        if (u == 0) O9 = mfma(af, p9, O9);
      }
    }
  };
  wait_bar<14>(); vstep(0);
  wait_bar<12>(); vstep(1);
  wait_bar<10>(); vstep(2);
  wait_bar<8>(); vstep(3);
  wait_bar<6>(); vstep(4);
  wait_bar<4>(); vstep(5);
  wait_bar<2>(); vstep(6);
  wait_bar<0>(); vstep(7);

  float l9t = 0.f;
#pragma unroll
  for (int i = 0; i < 8; ++i) l9t += *(const LAS float*)(size_t)(XCH + 512u + i * 64u + c * 4u);
  const float inv = 1.0f / ls, inv9 = 1.0f / l9t;
  char* ob = (char*)obuf + (size_t)(b * NH + h) * 36864u;
  const bool odd = (w & 1u) != 0;
#pragma unroll
  for (int s = 0; s < 4; ++s) {
    unsigned a0 = cvtpk(O[2 * s][0] * inv, O[2 * s][1] * inv), a1 = cvtpk(O[2 * s][2] * inv, O[2 * s][3] * inv);
    unsigned b0 = cvtpk(O[2 * s + 1][0] * inv, O[2 * s + 1][1] * inv), b1 = cvtpk(O[2 * s + 1][2] * inv, O[2 * s + 1][3] * inv);
    u32x4 f = odd ? (u32x4){b0, b1, a0, a1} : (u32x4){a0, a1, b0, b1};
    *(u32x4*)(ob + w * 4096u + ((s ^ (w >> 1)) << 10) + l * 16u) = f;
  }
  { u32x2 v = {cvtpk(O9[0] * inv9, O9[1] * inv9), cvtpk(O9[2] * inv9, O9[3] * inv9)};
    *(u32x2*)(ob + 8u * 4096u + ((w >> 1) << 10) + l * 16u + (w & 1u) * 8u) = v; }
}

__global__ __launch_bounds__(512, 2) void k2_oproj(const unsigned short* __restrict__ obuf, const unsigned short* __restrict__ wo, float* __restrict__ part) {
  extern __shared__ __attribute__((aligned(16))) char lds[];
  const unsigned lb = (unsigned)(size_t)lds;
  const unsigned tid = threadIdx.x, l = tid & 63u, c = l & 15u, G = l >> 4, q4 = (l >> 2) & 3u, p = l & 3u;
  const unsigned w = rfl(tid >> 6);
  const unsigned blk = blockIdx.x, g = blk & 7u, r_ = blk >> 3, j = r_ & 3u, b = r_ >> 2;
  const __amdgpu_buffer_rsrc_t srdO = __builtin_amdgcn_make_buffer_rsrc((void*)(wo + (size_t)g * 512 * DM + j * 144), (short)0, (4096 * DM - g * 512 * DM - j * 144) * 2, 0x00020000);
  unsigned vo[2];
#pragma unroll
  for (int e = 0; e < 2; ++e) { const unsigned ch = (2u * w + e) * 64u + l; vo[e] = (ch / 18u) * 1152u + (ch % 18u) * 16u; }
  unsigned vo9; { const unsigned dw = 4096u + w * 64u + l; vo9 = (dw / 72u) * 1152u + (dw % 72u) * 4u; }
  asm volatile("s_nop 4");
#pragma unroll
  for (int t = 0; t < 8; ++t) { const unsigned s = lb + t * SLOT;
    dma16(s + w * 2048u, vo[0], srdO, t * 64 * 1152); dma16(s + w * 2048u + 1024u, vo[1], srdO, t * 64 * 1152); dma4(s + 16384u + w * 256u, vo9, srdO, t * 64 * 1152); }
  const char* ob = (const char*)obuf + (size_t)(b * NH + 4 * g) * 36864u;
  bf16x8 bo[16], b9[16];
#pragma unroll
  for (int hh = 0; hh < 4; ++hh)
#pragma unroll
    for (int s = 0; s < 4; ++s) {
      bo[hh * 4 + s] = *(const bf16x8*)(ob + hh * 36864u + w * 4096u + s * 1024u + l * 16u);
      b9[hh * 4 + s] = *(const bf16x8*)(ob + hh * 36864u + 8u * 4096u + s * 1024u + l * 16u);
    }
  f32x4 acc[9], acc9 = {0.f, 0.f, 0.f, 0.f}, acc99 = {0.f, 0.f, 0.f, 0.f};
#pragma unroll
  for (int u = 0; u < 9; ++u) acc[u] = (f32x4){0.f, 0.f, 0.f, 0.f};
  const unsigned aob = (4u * G + q4) * 288u + 8u * p;
  asm volatile("s_waitcnt vmcnt(0)\n\ts_barrier" ::: "memory");
#pragma unroll
  for (int ks = 0; ks < 16; ++ks) {
    const unsigned sb = lb + (ks >> 1) * SLOT + (ks & 1) * 32u * 288u + aob;
#pragma unroll
    for (int u = 0; u < 9; ++u) {
      const unsigned a = sb + (u < 8 ? ((u ^ w) << 5) : 256u);
      const bf16x8 af = cat(ldstr(a), ldstr(a + 16u * 288u));
      acc[u] = mfma(af, bo[ks], acc[u]);
      if (u == 0) acc9 = mfma(af, b9[ks], acc9);
      if (u == 8) { if (w == 0) acc99 = mfma(af, b9[ks], acc99); }
    }
  }
  char* pb = (char*)part + (size_t)((b * 4 + j) * 8 + g) * 82944u;
#pragma unroll
  for (int u = 0; u < 9; ++u) { const unsigned nt = u < 8 ? (u ^ w) : 8u; *(f32x4*)(pb + (w * 9u + nt) * 1024u + l * 16u) = acc[u]; }
  *(f32x4*)(pb + (72u + w) * 1024u + l * 16u) = acc9;
  if (w == 0) *(f32x4*)(pb + 80u * 1024u + l * 16u) = acc99;
}

__global__ __launch_bounds__(256) void k3_reduce(const float* __restrict__ part, unsigned short* __restrict__ out) {
  const unsigned idx = blockIdx.x * 256u + threadIdx.x;
  const unsigned bj = idx / 5184u, ch = idx % 5184u, b = bj >> 2, j = bj & 3u;
  const char* pb = (const char*)part + (size_t)bj * 8u * 82944u + ch * 16u;
  f32x4 s = *(const f32x4*)pb;
#pragma unroll
  for (int g = 1; g < 8; ++g) s += *(const f32x4*)(pb + g * 82944u);
  const unsigned fr = ch >> 6, l = ch & 63u, qt = fr / 9u, nt = fr % 9u;
  const unsigned q = qt * 16u + (l & 15u), n = j * 144u + nt * 16u + (l >> 4) * 4u;
  u32x2 v = {cvtpk(s[0], s[1]), cvtpk(s[2], s[3])};
  *(u32x2*)(out + (size_t)(b * SQ + q) * DM + n) = v;
}

extern "C" void kernel_launch(void* const* d_in, const int* in_sizes, int n_in, void* d_out, int out_size, void* d_ws, size_t ws_size, hipStream_t stream) {
  const unsigned short* x = (const unsigned short*)d_in[0];
  const unsigned short* kc = (const unsigned short*)d_in[1];
  const unsigned short* vc = (const unsigned short*)d_in[2];
  const unsigned short* wq = (const unsigned short*)d_in[3];
  const unsigned short* wo = (const unsigned short*)d_in[4];
  unsigned short* obuf = (unsigned short*)d_ws;
  float* part = (float*)((char*)d_ws + (16u << 20));
  (void)hipFuncSetAttribute((const void*)k1_attn, hipFuncAttributeMaxDynamicSharedMemorySize, LDS_BYTES);
  (void)hipFuncSetAttribute((const void*)k2_oproj, hipFuncAttributeMaxDynamicSharedMemorySize, LDS_BYTES);
  k1_attn<<<256, 512, LDS_BYTES, stream>>>(x, kc, vc, wq, obuf);
  k2_oproj<<<256, 512, MISC, stream>>>(obuf, wo, part);
  k3_reduce<<<32 * 5184 / 256, 256, 0, stream>>>(part, (unsigned short*)d_out);
}
```

```cpp
#include <hip/hip_runtime.h>
#include <hip/hip_bf16.h>

#define LAS __attribute__((address_space(3)))
#define DI __device__ __forceinline__
typedef short bf16x8 __attribute__((ext_vector_type(8)));
typedef short s16x4 __attribute__((ext_vector_type(4)));
typedef float f32x4 __attribute__((ext_vector_type(4)));
typedef float f32x2 __attribute__((ext_vector_type(2)));
typedef __bf16 bf16x2 __attribute__((ext_vector_type(2)));
typedef unsigned u32x4 __attribute__((ext_vector_type(4)));
typedef unsigned u32x2 __attribute__((ext_vector_type(2)));

constexpr int SQ = 144, DM = 576, NH = 32, SK = 512, DH = 128;
constexpr unsigned SLOT = 18432, MISC = 8 * SLOT, LDS_BYTES = MISC + 16384;
#define THRP 1.8446744073709552e19f
constexpr size_t WS_OX = 0, WS_PX = (size_t)16 << 20, WS_FLAG = (size_t)64 << 20;
constexpr float SL2E = 0.08838834764831845f * 1.4426950408889634f;

DI unsigned rfl(unsigned x) { return __builtin_amdgcn_readfirstlane(x); }
DI unsigned cvtpk(float lo, float hi) { f32x2 v = {lo, hi}; bf16x2 b = __builtin_convertvector(v, bf16x2); return __builtin_bit_cast(unsigned, b); }
DI bf16x8 lds128(unsigned a) { return *(const LAS bf16x8*)(size_t)a; }
DI s16x4 ldstr(unsigned a) { return __builtin_amdgcn_ds_read_tr16_b64_v4i16((LAS s16x4*)(size_t)a); }
DI bf16x8 cat(s16x4 lo, s16x4 hi) { return __builtin_shufflevector(lo, hi, 0, 1, 2, 3, 4, 5, 6, 7); }
DI f32x4 mfma(bf16x8 a, bf16x8 b, f32x4 c) { return __builtin_amdgcn_mfma_f32_16x16x32_bf16(a, b, c, 0, 0, 0); }
#define SB() __builtin_amdgcn_sched_barrier(0)
DI unsigned opq(unsigned x) { asm volatile("" : "+v"(x)); return x; }
#define PIN(x) asm volatile("" : "+v"(x))
DI float max3f(float a, float b, float c) { float r; asm("v_max3_f32 %0, %1, %2, %3" : "=v"(r) : "v"(a), "v"(b), "v"(c)); return r; }
DI float max2f(float a, float b) { float r; asm("v_max_f32_e32 %0, %1, %2" : "=v"(r) : "v"(a), "v"(b)); return r; }

DI void dma16(unsigned ldsdst, unsigned voff, __amdgpu_buffer_rsrc_t srd, unsigned soff) {
  asm volatile("s_mov_b32 m0, %0\n\ts_nop 0\n\tbuffer_load_dwordx4 %1, %2, %3 offen lds" :: "s"(ldsdst), "v"(voff), "s"(srd), "s"(soff) : "memory");
}
DI void dma16nt(unsigned ldsdst, unsigned voff, __amdgpu_buffer_rsrc_t srd, unsigned soff) {
  asm volatile("s_mov_b32 m0, %0\n\ts_nop 0\n\tbuffer_load_dwordx4 %1, %2, %3 offen nt lds" :: "s"(ldsdst), "v"(voff), "s"(srd), "s"(soff) : "memory");
}
DI void dma4(unsigned ldsdst, unsigned voff, __amdgpu_buffer_rsrc_t srd, unsigned soff) {
  asm volatile("s_mov_b32 m0, %0\n\ts_nop 0\n\tbuffer_load_dword %1, %2, %3 offen lds" :: "s"(ldsdst), "v"(voff), "s"(srd), "s"(soff) : "memory");
}
template <int N> DI void wait_bar() { asm volatile("s_waitcnt vmcnt(%0)\n\ts_barrier" :: "i"(N) : "memory"); }
template <int N> DI void wait_bar_l() { asm volatile("s_waitcnt vmcnt(%0) lgkmcnt(0)\n\ts_barrier" :: "i"(N) : "memory"); }
DI void lds_bar() { asm volatile("s_waitcnt lgkmcnt(0)\n\ts_barrier" ::: "memory"); }

#define TS(i) do{}while(0)
#define ROT 1
#define KV_NT 1
DI void dmakv(unsigned ldsdst, unsigned voff, __amdgpu_buffer_rsrc_t srd, unsigned soff) {
  if (KV_NT) dma16nt(ldsdst, voff, srd, soff); else dma16(ldsdst, voff, srd, soff);
}

#define KSEG {1, 3, 5, 6, 7, 7, 8, 8}
struct Sched { int kseg[8]; };
constexpr Sched SC = {KSEG};
constexpr int nK(int s) { int n = 0; for (int t = 0; t < 8; ++t) n += SC.kseg[t] == s; return n; }
constexpr int cum(int s) { int n = 20; for (int i = 0; i <= s; ++i) n += ((i >= 2 && i <= 6) ? 5 : 0) + 2 * nK(i); return n; }
constexpr int posXW(int n) { return n <= 3 ? 5 * (n + 1) : cum(n - 3) + 5; }
constexpr int posK(int t) { int s = SC.kseg[t], n = cum(s - 1) + ((s >= 2 && s <= 6) ? 5 : 0); for (int i = 0; i <= t; ++i) n += (SC.kseg[i] == s) ? 2 : 0; return n; }
constexpr int cl63(int n) { return n > 63 ? 63 : n; }

DI void slow_tile_impl(bf16x8 q0, bf16x8 q1, bf16x8 q2, bf16x8 q3, const char* Kg, const unsigned short* Vg, __amdgpu_buffer_rsrc_t dsrd, unsigned dst, unsigned l) {
  const unsigned c = l & 15u, G = l >> 4;
  const f32x4 zero4 = {0.f, 0.f, 0.f, 0.f};
  float m = -3.0e38f;
#pragma nounroll
  for (int kt = 0; kt < 32; ++kt) {
    const char* kp = Kg + (16u * kt + c) * 256u + G * 16u;
    f32x4 a = mfma(*(const bf16x8*)kp, q0, zero4); a = mfma(*(const bf16x8*)(kp + 64), q1, a); a = mfma(*(const bf16x8*)(kp + 128), q2, a); a = mfma(*(const bf16x8*)(kp + 192), q3, a);
    m = fmaxf(fmaxf(m, fmaxf(a[0], a[1])), fmaxf(a[2], a[3]));
  }
  m = fmaxf(m, __shfl_xor(m, 16)); m = fmaxf(m, __shfl_xor(m, 32));
  f32x4 O[8];
#pragma unroll
  for (int u = 0; u < 8; ++u) O[u] = zero4;
  float ls = 0.f;
#pragma nounroll
  for (int ks = 0; ks < 16; ++ks) {
    unsigned pk[4];
#pragma unroll
    for (int hb = 0; hb < 2; ++hb) {
      const char* kp = Kg + (32u * ks + 16u * hb + c) * 256u + G * 16u;
      f32x4 a = mfma(*(const bf16x8*)kp, q0, zero4); a = mfma(*(const bf16x8*)(kp + 64), q1, a); a = mfma(*(const bf16x8*)(kp + 128), q2, a); a = mfma(*(const bf16x8*)(kp + 192), q3, a);
      const float e0 = __builtin_amdgcn_exp2f(a[0] - m), e1 = __builtin_amdgcn_exp2f(a[1] - m), e2 = __builtin_amdgcn_exp2f(a[2] - m), e3 = __builtin_amdgcn_exp2f(a[3] - m);
      ls += (e0 + e1) + (e2 + e3);
      pk[2 * hb] = cvtpk(e0, e1); pk[2 * hb + 1] = cvtpk(e2, e3);
    }
    const u32x4 pu = {pk[0], pk[1], pk[2], pk[3]};
    const unsigned short* vp0 = Vg + (32u * ks + 4u * G) * 128u + c; const unsigned short* vp1 = vp0 + 16 * 128;
#pragma unroll
    for (int u = 0; u < 8; ++u) {
      bf16x8 vf;
#pragma unroll
      for (int jj = 0; jj < 8; ++jj) vf[jj] = (short)(jj < 4 ? vp0 : vp1)[(jj & 3) * 128 + 16 * u];
      O[u] = mfma(vf, __builtin_bit_cast(bf16x8, pu), O[u]);
    }
  }
  ls += __shfl_xor(ls, 16); ls += __shfl_xor(ls, 32);
  const float inv = 1.0f / ls;
#pragma unroll
  for (int s = 0; s < 4; ++s) {
    u32x4 f = {cvtpk(O[2 * s][0] * inv, O[2 * s][1] * inv), cvtpk(O[2 * s][2] * inv, O[2 * s][3] * inv),
               cvtpk(O[2 * s + 1][0] * inv, O[2 * s + 1][1] * inv), cvtpk(O[2 * s + 1][2] * inv, O[2 * s + 1][3] * inv)};
    __builtin_amdgcn_raw_buffer_store_b128(f, dsrd, dst + s * 1024u + l * 16u, 0, 16);
  }
}

__global__ __launch_bounds__(512, 2) void k1_attn(const unsigned short* __restrict__ x, const unsigned short* __restrict__ kc,
                                                  const unsigned short* __restrict__ vc, const unsigned short* __restrict__ wq,
                                                  const unsigned short* __restrict__ wo, unsigned short* __restrict__ out, char* ws) {
  extern __shared__ __attribute__((aligned(16))) char lds[];
  TS(0);
  const unsigned lb = (unsigned)(size_t)lds;
  const unsigned tid = threadIdx.x, l = tid & 63u, c = l & 15u, G = l >> 4, q4 = (l >> 2) & 3u, p = l & 3u;
  const unsigned w = rfl(tid >> 6);
  const unsigned blk = blockIdx.x, g = blk & 7u, r_ = blk >> 3, j = r_ & 3u, b = r_ >> 2, h = 4u * g + j;
  const unsigned rot = ROT ? (b + 2u * j) % 9u : 0u;

  asm volatile("" : "+s"(x), "+s"(kc), "+s"(vc), "+s"(wq), "+s"(wo), "+s"(ws));
  const __amdgpu_buffer_rsrc_t srdF = __builtin_amdgcn_make_buffer_rsrc((void*)(ws + WS_FLAG), (short)0, 256 * 256, 0x00020000);
  const __amdgpu_buffer_rsrc_t srdOX = __builtin_amdgcn_make_buffer_rsrc((void*)(ws + WS_OX + (size_t)(b * NH + 4u * g) * 36864u), (short)0, 4 * 36864, 0x00020000);
  const __amdgpu_buffer_rsrc_t srdPX = __builtin_amdgcn_make_buffer_rsrc((void*)(ws + WS_PX + (size_t)(b * 4u + j) * 8u * 82944u), (short)0, 8 * 82944, 0x00020000);
  unsigned fadr;
  {
    const unsigned bj = (b * 4u + (l & 3u)) * 8u + g, bg = (b * 4u + j) * 8u + (l & 7u);
    if (l < 4u) fadr = bj * 256u;
    else if (l < 8u) fadr = bj * 256u + 64u + j * 4u;
    else if (l < 16u) fadr = bg * 256u + 32u;
    else if (l < 24u) fadr = bg * 256u + 128u + g * 4u;
    else if (l < 28u) fadr = blk * 256u + 64u + (l & 3u) * 4u;
    else if (l < 32u) fadr = blk * 256u;
    else if (l < 40u) fadr = blk * 256u + 128u + (l & 7u) * 4u;
    else fadr = blk * 256u;
  }
  unsigned finit;
  asm volatile("s_nop 4\n\tbuffer_load_dword %0, %1, %2, 0 offen sc1" : "=v"(finit) : "v"(fadr), "s"(srdF) : "memory");
  const __amdgpu_buffer_rsrc_t srdX = __builtin_amdgcn_make_buffer_rsrc((void*)(x + (size_t)b * SQ * DM), (short)0, SQ * DM * 2, 0x00020000);
  const __amdgpu_buffer_rsrc_t srdW = __builtin_amdgcn_make_buffer_rsrc((void*)(wq + (size_t)h * DH), (short)0, (DM * 4096 - h * DH) * 2, 0x00020000);
  const __amdgpu_buffer_rsrc_t srdK = __builtin_amdgcn_make_buffer_rsrc((void*)(kc + (size_t)(b * NH + h) * SK * DH), (short)0, SK * DH * 2, 0x00020000);
  const __amdgpu_buffer_rsrc_t srdV = __builtin_amdgcn_make_buffer_rsrc((void*)(vc + (size_t)(b * NH + h) * SK * DH), (short)0, SK * DH * 2, 0x00020000);

  const unsigned vx0 = (16u * w + (l >> 3)) * 1152u + (((l & 7u) ^ (G & 7u)) << 4);
  const unsigned vx1 = (16u * w + 8u + (l >> 3)) * 1152u + (((l & 7u) ^ ((4u + G) & 7u)) << 4);
  const unsigned vx9 = (128u + 2u * w + (l >> 5)) * 1152u + (((((l & 31u) >> 2) ^ w) & 7u) << 4) + ((l & 3u) << 2);
  const unsigned vw = (8u * w + G) * 8192u + ((((c >> 1) ^ (G | ((w & 1u) << 2)))) << 5) + ((c & 1u) << 4);
  const unsigned vkl = w * 2048u + l * 16u;
  const unsigned kw0 = lb + (8u * w + G) * 256u + ((c ^ (8u * (w & 1u) + G)) << 4);
  const unsigned kw1 = lb + (8u * w + 4u + G) * 256u + ((c ^ (8u * (w & 1u) + 4u + G)) << 4);
  const unsigned vv0 = (8u * w + G) * 256u + (((c >> 1) ^ G) << 5) + ((c & 1u) << 4);
  const unsigned vv1 = (8u * w + 4u + G) * 256u + (((c >> 1) ^ (4u + G)) << 5) + ((c & 1u) << 4);
  constexpr int KS[8] = {2, 3, 6, 2, 3, 6, 2, 3}, VS[8] = {7, 0, 1, 4, 5, 6, 2, 3};

  auto issueXW = [&](int i) { const unsigned s = lb + ((2 * i) & 7) * SLOT; unsigned kt = i + rot; kt = kt >= 9u ? kt - 9u : kt;
    dma16(s + w * 2048u, vx0, srdX, kt * 128u); dma16(s + w * 2048u + 1024u, vx1, srdX, kt * 128u); dma4(s + 16384u + w * 256u, vx9, srdX, kt * 128u);
    dma16(s + SLOT + w * 2048u, vw, srdW, kt * (64u * 8192u)); dma16(s + SLOT + w * 2048u + 1024u, vw, srdW, kt * (64u * 8192u) + 4u * 8192u); };
  u32x4 kp[8][2];
  auto loadK = [&](int t) {
    if (KV_NT) asm volatile("buffer_load_dwordx4 %0, %2, %3, %4 offen nt\n\tbuffer_load_dwordx4 %1, %2, %3, %4 offen offset:1024 nt" : "=&v"(kp[t][0]), "=&v"(kp[t][1]) : "v"(vkl), "s"(srdK), "s"(t * 16384) : "memory");
    else asm volatile("buffer_load_dwordx4 %0, %2, %3, %4 offen\n\tbuffer_load_dwordx4 %1, %2, %3, %4 offen offset:1024" : "=&v"(kp[t][0]), "=&v"(kp[t][1]) : "v"(vkl), "s"(srdK), "s"(t * 16384) : "memory");
  };
  auto writeK = [&](int t) {
    *(LAS u32x4*)(size_t)(kw0 + KS[t] * SLOT) = kp[t][0]; *(LAS u32x4*)(size_t)(kw1 + KS[t] * SLOT) = kp[t][1]; };
#define KWAIT(N, t) asm volatile("s_waitcnt vmcnt(%2)" : "+v"(kp[t][0]), "+v"(kp[t][1]) : "i"(N) : "memory")
  auto issueV = [&](int t) { const unsigned s = lb + VS[t] * SLOT + w * 2048u;
    dmakv(s, vv0, srdV, t * 16384); dmakv(s + 1024u, vv1, srdV, t * 16384); };

  asm volatile("s_nop 4");
  issueXW(0); issueXW(1); issueXW(2); issueXW(3);

  const unsigned XCH = lb + 16384u;
  const unsigned qg = w & 3u, hf = w >> 2, wv = 2u * qg + hf;
  const f32x4 zero4 = {0.f, 0.f, 0.f, 0.f};
  {
    f32x4 qa[2][4], qa9 = zero4;
#pragma unroll
    for (int t = 0; t < 4; ++t) { qa[0][t] = zero4; qa[1][t] = zero4; }
    const unsigned axo = lb + (32u * qg + c) * 128u + ((G ^ (c >> 1)) << 4);
    const unsigned ax9 = lb + (128u + c) * 128u + ((G ^ (c >> 1)) << 4);
    const unsigned awb = (8u * G + q4) * 256u + (((4u * hf + qg) ^ (q4 | ((G & 1u) << 2))) << 5) + 8u * p;
    bf16x8 af[2][4], bx[2][3];
    unsigned wb[2][4], xb[2][2], x9b[2][2];
#pragma unroll
    for (int hh = 0; hh < 2; ++hh) {
#pragma unroll
      for (int t = 0; t < 4; ++t) wb[hh][t] = opq(lb + (awb ^ (t << 5)) + hh * 4 * SLOT);
#pragma unroll
      for (int ks = 0; ks < 2; ++ks) { xb[hh][ks] = opq((axo ^ (ks << 6)) + hh * 4 * SLOT); x9b[hh][ks] = opq((ax9 ^ (ks << 6)) + hh * 4 * SLOT); }
    }
    auto rdA = [&](int gg, int t) { const int i = gg >> 1, ks = gg & 1, sl = (2 * i + 1) & 7;
      const unsigned a = wb[sl >> 2][t] + (sl & 3) * SLOT + ks * 8192;
      af[gg & 1][t] = cat(ldstr(a), ldstr(a + 1024)); };
    auto rdB = [&](int gg, int e) { const int i = gg >> 1, ks = gg & 1, sl = (2 * i) & 7;
      if (e < 2) bx[gg & 1][e] = lds128(xb[sl >> 2][ks] + (sl & 3) * SLOT + e * 2048); else bx[gg & 1][2] = lds128(x9b[sl >> 2][ks] + (sl & 3) * SLOT); };
    auto grp = [&](int gg) {
#pragma unroll
      for (int t = 0; t < 4; ++t) {
        qa[0][t] = mfma(af[gg & 1][t], bx[gg & 1][0], qa[0][t]);
        qa[1][t] = mfma(af[gg & 1][t], bx[gg & 1][1], qa[1][t]);
        if (t == 0) { qa9 = mfma(af[gg & 1][0], bx[gg & 1][2], qa9); PIN(qa9); }
        if (gg < 17) { if (t < 3) rdB(gg + 1, t); rdA(gg + 1, t); }
        SB();
      }
    };
    asm volatile("s_waitcnt vmcnt(15)\n\ts_barrier" : "+v"(finit) :: "memory"); TS(1);
    rdB(0, 0); rdB(0, 1); rdB(0, 2);
#pragma unroll
    for (int t = 0; t < 4; ++t) rdA(0, t);
    SB();
#define LK(sg) do { _Pragma("unroll") for (int t_ = 0; t_ < 8; ++t_) if (SC.kseg[t_] == (sg)) loadK(t_); } while (0)
    grp(0);  LK(0); wait_bar<cum(0) - posXW(1)>(); TS(2);
    grp(1);  grp(2);  LK(1); wait_bar<cum(1) - posXW(2)>(); TS(3);
    grp(3);  grp(4);  issueXW(4); LK(2); wait_bar<cum(2) - posXW(3)>(); TS(4);
    grp(5);  grp(6);  issueXW(5); LK(3); wait_bar<cum(3) - posXW(4)>(); TS(5);
    grp(7);  grp(8);  issueXW(6); LK(4); wait_bar<cum(4) - posXW(5)>(); TS(6);
    grp(9);  grp(10); issueXW(7); LK(5); wait_bar<cum(5) - posXW(6)>(); TS(7);
    grp(11); grp(12); issueXW(8); LK(6); wait_bar<cum(6) - posXW(7)>(); TS(8);
    grp(13); grp(14); LK(7); wait_bar<cum(7) - posXW(8)>(); TS(9);
    grp(15); grp(16); grp(17); LK(8);
#undef LK
    KWAIT(cl63(cum(8) - posK(0)), 0); writeK(0); KWAIT(cl63(cum(8) - posK(1)), 1); writeK(1);
    const unsigned QB = lb + 4u * SLOT;
#pragma unroll
    for (int e = 0; e < 2; ++e)
#pragma unroll
      for (int t = 0; t < 4; ++t) {
        const unsigned dt = 4u * hf + (t ^ qg);
        u32x2 v = {cvtpk(qa[e][t][0] * SL2E, qa[e][t][1] * SL2E), cvtpk(qa[e][t][2] * SL2E, qa[e][t][3] * SL2E)};
        *(LAS u32x2*)(size_t)(QB + (32u * qg + 16u * e + c) * 256u + (((2u * dt + (G >> 1)) ^ c) << 4) + (G & 1u) * 8u) = v;
      }
    { const unsigned dt = 4u * hf + qg;
      u32x2 v = {cvtpk(qa9[0] * SL2E, qa9[1] * SL2E), cvtpk(qa9[2] * SL2E, qa9[3] * SL2E)};
      *(LAS u32x2*)(size_t)(lb + MISC + c * 256u + (((2u * dt + (G >> 1)) ^ c) << 4) + (G & 1u) * 8u) = v; }
  }

  u32x4 P[8][2];
  f32x4 lsum[2] = {zero4, zero4};
  float nref[2];
  unsigned slow;
  {
    bf16x8 qf[2][4], q9f[4], kf[3][4];
    f32x4 S[2][2][2];
    f32x4 ncv[2] = {zero4, zero4}, nc9 = zero4, S9f = zero4;
    float l9 = 0.f;
    const u32x4 onesu = {0x3F803F80u, 0x3F803F80u, 0x3F803F80u, 0x3F803F80u};
    const bf16x8 ones = __builtin_bit_cast(bf16x8, onesu);
    const unsigned tpar = (qg >> 1) ^ hf;
    const unsigned dutymask = (0x1111u << (2u * tpar + (qg & 1u)));
    const unsigned p9w = opq(lb + MISC + hf * 1024u + l * 16u + (qg & 1u) * 8u);
    const unsigned akb = lb + c * 256u + ((G ^ c) << 4) + hf * 8192u;
    unsigned kb[2][4];
#pragma unroll
    for (int hh = 0; hh < 2; ++hh)
#pragma unroll
      for (int s = 0; s < 4; ++s) kb[hh][s] = opq((akb ^ (s << 6)) + hh * 4 * SLOT);
    auto rdK = [&](int un) { const int sl = KS[un >> 1];
#pragma unroll
      for (int s = 0; s < 4; ++s) kf[un % 3][s] = lds128(kb[sl >> 2][s] + (sl & 3) * SLOT + (un & 1) * 4096); };
    auto rowmax = [&](float m) {
      { auto r = __builtin_amdgcn_permlane32_swap(__float_as_uint(m), __float_as_uint(m), false, false); m = fmaxf(__uint_as_float(r[0]), __uint_as_float(r[1])); }
      { auto r = __builtin_amdgcn_permlane16_swap(__float_as_uint(m), __float_as_uint(m), false, false); m = fmaxf(__uint_as_float(r[0]), __uint_as_float(r[1])); }
      return m; };
    auto expack = [&](int T, int e) {
      const f32x4 a = S[T & 1][0][e], bq = S[T & 1][1][e];
      float e0 = __builtin_amdgcn_exp2f(a[0]), e1 = __builtin_amdgcn_exp2f(a[1]), e2 = __builtin_amdgcn_exp2f(a[2]), e3 = __builtin_amdgcn_exp2f(a[3]);
      float e4 = __builtin_amdgcn_exp2f(bq[0]), e5 = __builtin_amdgcn_exp2f(bq[1]), e6 = __builtin_amdgcn_exp2f(bq[2]), e7 = __builtin_amdgcn_exp2f(bq[3]);
      u32x4 f = {cvtpk(e0, e1), cvtpk(e2, e3), cvtpk(e4, e5), cvtpk(e6, e7)};
      P[T][e] = f;
      lsum[e] = mfma(ones, __builtin_bit_cast(bf16x8, f), lsum[e]);
      PIN(P[T][e]); PIN(lsum[e]);
    };
    auto exp9 = [&](f32x4 s, unsigned dst) {
      const float e0 = __builtin_amdgcn_exp2f(s[0]), e1 = __builtin_amdgcn_exp2f(s[1]), e2 = __builtin_amdgcn_exp2f(s[2]), e3 = __builtin_amdgcn_exp2f(s[3]);
      l9 += (e0 + e1) + (e2 + e3);
      u32x2 v = {cvtpk(e0, e1), cvtpk(e2, e3)};
      *(LAS u32x2*)(size_t)dst = v; };
    auto setref = [&]() {
#pragma unroll
      for (int e = 0; e < 2; ++e) {
        f32x4 (&Sp)[2][2] = S[0];
        float d = fmaxf(fmaxf(fmaxf(Sp[0][e][0], Sp[0][e][1]), fmaxf(Sp[0][e][2], Sp[0][e][3])), fmaxf(fmaxf(Sp[1][e][0], Sp[1][e][1]), fmaxf(Sp[1][e][2], Sp[1][e][3])));
        d = rowmax(d);
        Sp[0][e] -= d; Sp[1][e] -= d; ncv[e] -= d;
      }
      SB();
    };
    auto kunit = [&](int un) {
      const int t = un >> 1, i = un & 1;
      if (un + 2 < 16) rdK(un + 2);
      SB();
      f32x4 a0 = ncv[0], a1 = ncv[1];
#pragma unroll
      for (int s = 0; s < 4; ++s) { a0 = mfma(kf[un % 3][s], qf[0][s], a0); a1 = mfma(kf[un % 3][s], qf[1][s], a1); }
      S[t & 1][i][0] = a0; S[t & 1][i][1] = a1;
      if (t > 0) expack(t - 1, i);
      SB();
      if (dutymask & (1u << un)) {
        f32x4 acc9 = nc9;
#pragma unroll
        for (int s = 0; s < 4; ++s) acc9 = mfma(kf[un % 3][s], q9f[s], acc9);
        if (t < 2) {
          S9f = acc9;
          const float m = rowmax(fmaxf(fmaxf(acc9[0], acc9[1]), fmaxf(acc9[2], acc9[3])));
          if (G == 0) *(LAS float*)(size_t)(XCH + w * 64u + c * 4u) = m;
        } else exp9(acc9, p9w + t * 2048u);
      }
      SB();
      if (un == 1) setref();
    };
    lds_bar(); TS(10);
    {
      const unsigned qb = lb + 4u * SLOT + c * 256u;
#pragma unroll
      for (int s = 0; s < 4; ++s) {
        const unsigned o = (((4u * s + G) ^ c) << 4);
        qf[0][s] = lds128(qb + (2u * qg + hf) * 4096u + o);
        qf[1][s] = lds128(qb + (2u * qg + (1u - hf)) * 4096u + o);
        q9f[s] = lds128(lb + MISC + c * 256u + o);
      }
    }
    rdK(0); rdK(1);
    KWAIT(cl63(cum(8) - posK(2)), 2); writeK(2); SB();
    kunit(0); kunit(1); issueV(0); issueV(1); issueV(2);
    lds_bar(); TS(11); KWAIT(cl63(cum(8) + 6 - posK(3)), 3); writeK(3); kunit(2); kunit(3); issueV(3); issueV(4);
    lds_bar(); TS(12); KWAIT(cl63(cum(8) + 10 - posK(4)), 4); writeK(4);
    {
      float m = -3.0e38f;
#pragma unroll
      for (int i = 0; i < 8; ++i) m = fmaxf(m, *(const LAS float*)(size_t)(XCH + i * 64u + c * 4u));
      nc9 = (f32x4){-m, -m, -m, -m};
      exp9(S9f + nc9, p9w + tpar * 2048u);
      SB();
    }
    kunit(4); kunit(5);
    lds_bar(); TS(13); KWAIT(cl63(cum(8) + 10 - posK(5)), 5); writeK(5); kunit(6); kunit(7);
    lds_bar(); TS(14); KWAIT(cl63(cum(8) + 10 - posK(6)), 6); writeK(6); kunit(8); kunit(9);
    lds_bar(); TS(15); KWAIT(cl63(cum(8) + 10 - posK(7)), 7); writeK(7); kunit(10); kunit(11);
    lds_bar(); TS(16); kunit(12); kunit(13); issueV(5);
    wait_bar<63>(); TS(17); kunit(14); kunit(15); issueV(6);
    expack(7, 0); expack(7, 1);
    l9 += __shfl_xor(l9, 16); l9 += __shfl_xor(l9, 32);
    if (G == 0) *(LAS float*)(size_t)(XCH + 512u + w * 64u + c * 4u) = l9;
    {
      const bool bad = __any(!(lsum[0][0] <= THRP) || !(lsum[1][0] <= THRP) || !(l9 <= THRP));
      if (l == 0) *(LAS unsigned*)(size_t)(XCH + 1024u + w * 4u) = bad ? 1u : 0u; }
    wait_bar_l<12>(); TS(18);
    issueV(7);
    {
      const unsigned fl = *(const LAS unsigned*)(size_t)(XCH + 1024u + (l & 7u) * 4u);
      slow = rfl(__any(fl != 0u) ? 1u : 0u);
      if (__builtin_expect(slow != 0u, 0)) {
        const char* Kg = (const char*)(kc + (size_t)(b * NH + h) * SK * DH); const unsigned short* Vg = vc + (size_t)(b * NH + h) * SK * DH;
#pragma nounroll
        for (unsigned rep = 0; rep < (w == 0 ? 2u : 1u); ++rep)
          slow_tile_impl(rep ? q9f[0] : qf[0][0], rep ? q9f[1] : qf[0][1], rep ? q9f[2] : qf[0][2], rep ? q9f[3] : qf[0][3], Kg, Vg, srdOX, j * 36864u + (rep ? 8u : wv) * 4096u, l);
      }
    }
    nref[0] = ncv[0][0]; nref[1] = ncv[1][0];
  }
  SB();

  if (slow == 0u) {
  f32x4 O[2][8], O9[2] = {zero4, zero4};
#pragma unroll
  for (int u = 0; u < 8; ++u) { O[0][u] = zero4; O[1][u] = zero4; }
  {
    constexpr int PD = 6;
    const unsigned avb = (32u * hf + 4u * G + q4) * 256u + ((wv ^ (4u * (G & 1u) + q4)) << 5) + 8u * p;
    bf16x8 vf[8], p9[2];
    unsigned vb[2][8];
#pragma unroll
    for (int hh = 0; hh < 2; ++hh)
#pragma unroll
      for (int u = 0; u < 8; ++u) vb[hh][u] = opq(lb + (avb ^ (u << 5)) + hh * 4 * SLOT);
    const unsigned p9b = opq(lb + MISC + hf * 1024u + l * 16u);
    auto rdV = [&](int f) { const int t = f >> 3, u = f & 7, sl = VS[t];
      const unsigned a = vb[sl >> 2][u] + (sl & 3) * SLOT;
      vf[f & 7] = cat(ldstr(a), ldstr(a + 4096));
      if (u == 0) p9[t & 1] = lds128(p9b + t * 2048u); };
    auto vfr = [&](int f) { const int t = f >> 3, u = f & 7;
      O[0][u] = mfma(vf[f & 7], __builtin_bit_cast(bf16x8, P[t][0]), O[0][u]);
      O[1][u] = mfma(vf[f & 7], __builtin_bit_cast(bf16x8, P[t][1]), O[1][u]);
      if (u < 2) { O9[u] = mfma(vf[f & 7], p9[t & 1], O9[u]); PIN(O9[u]); }
      if (f + PD < 64) rdV(f + PD);
      SB(); };
#pragma unroll
    for (int f = 0; f < PD; ++f) rdV(f);
    SB(); TS(19);
#define VT(t, N, ts) vfr(8 * t); vfr(8 * t + 1); wait_bar<N>(); TS(ts); vfr(8 * t + 2); vfr(8 * t + 3); vfr(8 * t + 4); vfr(8 * t + 5); vfr(8 * t + 6); vfr(8 * t + 7);
    VT(0, 12, 20) VT(1, 10, 21) VT(2, 8, 22) VT(3, 6, 23) VT(4, 4, 24) VT(5, 2, 25) VT(6, 0, 26)
#undef VT
#pragma unroll
    for (int f = 56; f < 64; ++f) vfr(f);
  }
  TS(27);

  {
    const unsigned mine = lb + (w < 7u ? 4u * SLOT + w * 10240u : SLOT);
#pragma unroll
    for (int u = 0; u < 8; ++u) *(LAS f32x4*)(size_t)(mine + ((u ^ wv) << 10) + l * 16u) = O[1][u];
    *(LAS f32x4*)(size_t)(mine + 8192u + l * 16u) = O9[1];
    f32x2 st = {nref[1], lsum[1][0]};
    *(LAS f32x2*)(size_t)(mine + 9216u + l * 8u) = st;
  }
  lds_bar();
  float inv, inv9, fa, fb;
  {
    const unsigned wo_ = w ^ 4u;
    const unsigned oth = lb + (wo_ < 7u ? 4u * SLOT + wo_ * 10240u : SLOT);
    const f32x2 st = *(const LAS f32x2*)(size_t)(oth + 9216u + l * 8u);
    const float na = nref[0], nb = st[0], nm = fminf(na, nb);
    fa = __builtin_amdgcn_exp2f(nm - na); fb = __builtin_amdgcn_exp2f(nm - nb);
    inv = 1.0f / (lsum[0][0] * fa + st[1] * fb);
    fa *= inv; fb *= inv;
#pragma unroll
    for (int u = 0; u < 8; ++u) { const f32x4 o = *(const LAS f32x4*)(size_t)(oth + ((u ^ wv) << 10) + l * 16u); O[0][u] = O[0][u] * fa + o * fb; }
    O9[0] += *(const LAS f32x4*)(size_t)(oth + 8192u + l * 16u);
    float l9t = 0.f;
#pragma unroll
    for (int i = 0; i < 8; ++i) l9t += *(const LAS float*)(size_t)(XCH + 512u + i * 64u + c * 4u);
    inv9 = 1.0f / l9t;
  }
  const bool odd = (wv & 1u) != 0;
#pragma unroll
  for (int s = 0; s < 4; ++s) {
    unsigned a0 = cvtpk(O[0][2 * s][0], O[0][2 * s][1]), a1 = cvtpk(O[0][2 * s][2], O[0][2 * s][3]);
    unsigned b0 = cvtpk(O[0][2 * s + 1][0], O[0][2 * s + 1][1]), b1 = cvtpk(O[0][2 * s + 1][2], O[0][2 * s + 1][3]);
    u32x4 f = odd ? (u32x4){b0, b1, a0, a1} : (u32x4){a0, a1, b0, b1};
    __builtin_amdgcn_raw_buffer_store_b128(f, srdOX, j * 36864u + wv * 4096u + ((s ^ (wv >> 1)) << 10) + l * 16u, 0, 16);
  }
  { u32x2 v = {cvtpk(O9[0][0] * inv9, O9[0][1] * inv9), cvtpk(O9[0][2] * inv9, O9[0][3] * inv9)};
    __builtin_amdgcn_raw_buffer_store_b64(v, srdOX, j * 36864u + 8u * 4096u + ((wv >> 1) << 10) + l * 16u + (wv & 1u) * 8u, 0, 16); }
  }
  TS(28);

  asm volatile("s_waitcnt vmcnt(0) lgkmcnt(0)\n\ts_barrier" : "+v"(finit) :: "memory");
  TS(29);
  unsigned hb = 1u;
  auto poll = [&](int stage) {
    const unsigned long long self1 = (1ull << j) | (1ull << (4u + j)), self2 = (1ull << (8u + g)) | (1ull << (16u + g));
    for (unsigned it = 0; it < 400000u; ++it) {
      unsigned v;
      asm volatile("buffer_load_dword %0, %1, %2, 0 offen sc1\n\ts_waitcnt vmcnt(0)" : "=v"(v) : "v"(fadr), "s"(srdF) : "memory");
      const unsigned long long ch = __ballot(v != finit) | self1 | self2;
      const bool ack1 = (ch & 0xF0ull) == 0xF0ull, ack2 = (ch & 0xFF0000ull) == 0xFF0000ull;
      if (stage == 1 && (ch & 0xFull) == 0xFull) break;
      if (stage == 2 && (ch & 0xFF00ull) == 0xFF00ull) break;
      if (stage == 3 && ack1 && ack2) break;
      if ((it & 63u) == 63u) {
        ++hb;
        if ((l == j && !ack1) || (stage >= 2 && l == 8u + g && !ack2)) __builtin_amdgcn_raw_buffer_store_b32(finit + hb, srdF, fadr, 0, 16);
      }
      __builtin_amdgcn_s_sleep(2);
    }
  };
  if (w == 0 && l == j) __builtin_amdgcn_raw_buffer_store_b32(finit + 1u, srdF, fadr, 0, 16);
  {
    const __amdgpu_buffer_rsrc_t srdO = __builtin_amdgcn_make_buffer_rsrc((void*)(wo + (size_t)g * 512 * DM + j * 144), (short)0, (4096 * DM - g * 512 * DM - j * 144) * 2, 0x00020000);
    unsigned vo[2];
#pragma unroll
    for (int e = 0; e < 2; ++e) { const unsigned ch = (2u * w + e) * 64u + l; vo[e] = (ch / 18u) * 1152u + (ch % 18u) * 16u; }
    unsigned vo9; { const unsigned dw = 4096u + w * 64u + l; vo9 = (dw / 72u) * 1152u + (dw % 72u) * 4u; }
    asm volatile("s_nop 4");
#pragma unroll
    for (int t = 0; t < 8; ++t) { const unsigned s = lb + t * SLOT;
      dma16(s + w * 2048u, vo[0], srdO, t * 64 * 1152); dma16(s + w * 2048u + 1024u, vo[1], srdO, t * 64 * 1152); dma4(s + 16384u + w * 256u, vo9, srdO, t * 64 * 1152); }
  }
  if (w == 0) {
    poll(1);
    if (l >= 24u && l < 28u) __builtin_amdgcn_raw_buffer_store_b32(finit + 1u, srdF, fadr, 0, 16);
  }
  asm volatile("s_waitcnt vmcnt(0)\n\ts_barrier" ::: "memory");
  TS(30);
  f32x4 acc[9], acc9 = zero4, acc99 = zero4;
  {
    bf16x8 bo[16], b9[16];
#pragma unroll
    for (int i = 0; i < 16; ++i) bo[i] = __builtin_bit_cast(bf16x8, __builtin_amdgcn_raw_buffer_load_b128(srdOX, w * 4096u + l * 16u, (i >> 2) * 36864 + (i & 3) * 1024, 16));
#pragma unroll
    for (int i = 0; i < 16; ++i) b9[i] = __builtin_bit_cast(bf16x8, __builtin_amdgcn_raw_buffer_load_b128(srdOX, 8u * 4096u + l * 16u, (i >> 2) * 36864 + (i & 3) * 1024, 16));
#pragma unroll
    for (int u = 0; u < 9; ++u) acc[u] = zero4;
    const unsigned aob = lb + (4u * G + q4) * 288u + 8u * p;
    unsigned ab[2]; ab[0] = opq(aob); ab[1] = opq(aob + 4u * SLOT);
    bf16x8 af[2][9];
    auto rdA = [&](int ks, int u) { const int sl = ks >> 1; const unsigned a = ab[sl >> 2] + (sl & 3) * SLOT + (ks & 1) * 32u * 288u + u * 32u;
      af[ks & 1][u] = cat(ldstr(a), ldstr(a + 16u * 288u)); };
#pragma unroll
    for (int u = 0; u < 9; ++u) rdA(0, u);
    SB();
#pragma unroll
    for (int ks = 0; ks < 16; ++ks) {
#pragma unroll
      for (int u = 0; u < 9; ++u) {
        acc[u] = mfma(af[ks & 1][u], bo[ks], acc[u]);
        if (ks < 15) rdA(ks + 1, u);
        SB();
      }
    }
    const unsigned a9 = aob + w * 32u;
#pragma unroll
    for (int ks = 0; ks < 16; ++ks) { const unsigned a = a9 + (ks >> 1) * SLOT + (ks & 1) * 32u * 288u;
      acc9 = mfma(cat(ldstr(a), ldstr(a + 16u * 288u)), b9[ks], acc9); }
    if (w == 0) {
#pragma unroll
      for (int ks = 0; ks < 16; ++ks) { const unsigned a = aob + 256u + (ks >> 1) * SLOT + (ks & 1) * 32u * 288u;
        acc99 = mfma(cat(ldstr(a), ldstr(a + 16u * 288u)), b9[ks], acc99); }
    }
  }
  TS(31);
#pragma unroll
  for (int u = 0; u < 9; ++u) __builtin_amdgcn_raw_buffer_store_b128(__builtin_bit_cast(u32x4, acc[u]), srdPX, g * 82944u + (w * 9u + u) * 1024u + l * 16u, 0, 16);
  __builtin_amdgcn_raw_buffer_store_b128(__builtin_bit_cast(u32x4, acc9), srdPX, g * 82944u + (72u + w) * 1024u + l * 16u, 0, 16);
  if (w == 0) __builtin_amdgcn_raw_buffer_store_b128(__builtin_bit_cast(u32x4, acc99), srdPX, g * 82944u + 80u * 1024u + l * 16u, 0, 16);
  asm volatile("s_waitcnt vmcnt(0)\n\ts_barrier" ::: "memory");
  TS(32);
  if (w == 0) {
    if (l == 8u + g) __builtin_amdgcn_raw_buffer_store_b32(finit + 1u, srdF, fadr, 0, 16);
    poll(2);
    if (l >= 32u && l < 40u) __builtin_amdgcn_raw_buffer_store_b32(finit + 1u, srdF, fadr, 0, 16);
  }
  asm volatile("s_barrier" ::: "memory");
  TS(33);
#pragma unroll
  for (int r = 0; r < 2; ++r) {
    const unsigned fr = g + 8u * (w + 8u * r);
    if (fr < 81u) {
      f32x4 pv[8];
#pragma unroll
      for (int gg = 0; gg < 8; ++gg) pv[gg] = __builtin_bit_cast(f32x4, __builtin_amdgcn_raw_buffer_load_b128(srdPX, fr * 1024u + l * 16u, gg * 82944, 16));
      const f32x4 sm = ((pv[0] + pv[1]) + (pv[2] + pv[3])) + ((pv[4] + pv[5]) + (pv[6] + pv[7]));
      const unsigned qt = fr / 9u, nt = fr % 9u;
      u32x2 v = {cvtpk(sm[0], sm[1]), cvtpk(sm[2], sm[3])};
      *(u32x2*)(out + (size_t)(b * SQ + qt * 16u + c) * DM + j * 144u + nt * 16u + G * 4u) = v;
    }
  }
  TS(34);
  if (w == 0) poll(3);
}

extern "C" void kernel_launch(void* const* d_in, const int* in_sizes, int n_in, void* d_out, int out_size, void* d_ws, size_t ws_size, hipStream_t stream) {
  const unsigned short* x = (const unsigned short*)d_in[0];
  const unsigned short* kc = (const unsigned short*)d_in[1];
  const unsigned short* vc = (const unsigned short*)d_in[2];
  const unsigned short* wq = (const unsigned short*)d_in[3];
  const unsigned short* wo = (const unsigned short*)d_in[4];
  (void)hipFuncSetAttribute((const void*)k1_attn, hipFuncAttributeMaxDynamicSharedMemorySize, LDS_BYTES);
  k1_attn<<<256, 512, LDS_BYTES, stream>>>(x, kc, vc, wq, wo, (unsigned short*)d_out, (char*)d_ws);
}
```

```cpp
#include <hip/hip_runtime.h>
#include <hip/hip_bf16.h>

#define LAS __attribute__((address_space(3)))
#define DI __device__ __forceinline__
typedef short bf16x8 __attribute__((ext_vector_type(8)));
typedef short s16x4 __attribute__((ext_vector_type(4)));
typedef float f32x4 __attribute__((ext_vector_type(4)));
typedef float f32x2 __attribute__((ext_vector_type(2)));
typedef __bf16 bf16x2 __attribute__((ext_vector_type(2)));
typedef unsigned u32x4 __attribute__((ext_vector_type(4)));
typedef unsigned u32x2 __attribute__((ext_vector_type(2)));

constexpr int SQ = 144, DM = 576, NH = 32, SK = 512, DH = 128;
constexpr unsigned SLOT = 18432, MISC = 8 * SLOT, LDS_BYTES = MISC + 16384;
#define THRP 1.8446744073709552e19f
constexpr unsigned PXB = 41 * 1024;
constexpr size_t WS_OX = 0, WS_PX = (size_t)16 << 20, WS_FLAG = (size_t)64 << 20;
constexpr float SL2E = 0.08838834764831845f * 1.4426950408889634f;

DI unsigned rfl(unsigned x) { return __builtin_amdgcn_readfirstlane(x); }
DI unsigned cvtpk(float lo, float hi) { f32x2 v = {lo, hi}; bf16x2 b = __builtin_convertvector(v, bf16x2); return __builtin_bit_cast(unsigned, b); }
DI bf16x8 lds128(unsigned a) { return *(const LAS bf16x8*)(size_t)a; }
DI s16x4 ldstr(unsigned a) { return __builtin_amdgcn_ds_read_tr16_b64_v4i16((LAS s16x4*)(size_t)a); }
DI bf16x8 cat(s16x4 lo, s16x4 hi) { return __builtin_shufflevector(lo, hi, 0, 1, 2, 3, 4, 5, 6, 7); }
DI f32x4 mfma(bf16x8 a, bf16x8 b, f32x4 c) { return __builtin_amdgcn_mfma_f32_16x16x32_bf16(a, b, c, 0, 0, 0); }
#define SB() __builtin_amdgcn_sched_barrier(0)
DI unsigned opq(unsigned x) { asm volatile("" : "+v"(x)); return x; }
#define PIN(x) asm volatile("" : "+v"(x))
DI float max3f(float a, float b, float c) { float r; asm("v_max3_f32 %0, %1, %2, %3" : "=v"(r) : "v"(a), "v"(b), "v"(c)); return r; }
DI float max2f(float a, float b) { float r; asm("v_max_f32_e32 %0, %1, %2" : "=v"(r) : "v"(a), "v"(b)); return r; }

DI void dma16(unsigned ldsdst, unsigned voff, __amdgpu_buffer_rsrc_t srd, unsigned soff) {
  asm volatile("s_mov_b32 m0, %0\n\ts_nop 0\n\tbuffer_load_dwordx4 %1, %2, %3 offen lds" :: "s"(ldsdst), "v"(voff), "s"(srd), "s"(soff) : "memory");
}
DI void dma16nt(unsigned ldsdst, unsigned voff, __amdgpu_buffer_rsrc_t srd, unsigned soff) {
  asm volatile("s_mov_b32 m0, %0\n\ts_nop 0\n\tbuffer_load_dwordx4 %1, %2, %3 offen nt lds" :: "s"(ldsdst), "v"(voff), "s"(srd), "s"(soff) : "memory");
}
DI void dma4(unsigned ldsdst, unsigned voff, __amdgpu_buffer_rsrc_t srd, unsigned soff) {
  asm volatile("s_mov_b32 m0, %0\n\ts_nop 0\n\tbuffer_load_dword %1, %2, %3 offen lds" :: "s"(ldsdst), "v"(voff), "s"(srd), "s"(soff) : "memory");
}
template <int N> DI void wait_bar() { asm volatile("s_waitcnt vmcnt(%0)\n\ts_barrier" :: "i"(N) : "memory"); }
template <int N> DI void wait_bar_l() { asm volatile("s_waitcnt vmcnt(%0) lgkmcnt(0)\n\ts_barrier" :: "i"(N) : "memory"); }
DI void lds_bar4() { asm volatile("s_waitcnt lgkmcnt(4)\n\ts_barrier" ::: "memory"); }
DI void lds_bar() { asm volatile("s_waitcnt lgkmcnt(0)\n\ts_barrier" ::: "memory"); }

#define TS(i) do{}while(0)
#define ROT 1
#define KV_NT 0
DI void dmakv(unsigned ldsdst, unsigned voff, __amdgpu_buffer_rsrc_t srd, unsigned soff) {
  if (KV_NT) dma16nt(ldsdst, voff, srd, soff); else dma16(ldsdst, voff, srd, soff);
}

#define KSEG {1, 3, 5, 6, 7, 7, 8, 8}
struct Sched { int kseg[8]; };
constexpr Sched SC = {KSEG};
constexpr int nK(int s) { int n = 0; for (int t = 0; t < 8; ++t) n += SC.kseg[t] == s; return n; }
constexpr int cum(int s) { int n = 20; for (int i = 0; i <= s; ++i) n += ((i >= 2 && i <= 6) ? 5 : 0) + 2 * nK(i); return n; }
constexpr int posXW(int n) { return n <= 3 ? 5 * (n + 1) : cum(n - 3) + 5; }
constexpr int posK(int t) { int s = SC.kseg[t], n = cum(s - 1) + ((s >= 2 && s <= 6) ? 5 : 0); for (int i = 0; i <= t; ++i) n += (SC.kseg[i] == s) ? 2 : 0; return n; }
constexpr int cl63(int n) { return n > 63 ? 63 : n; }

DI void slow_tile_impl(bf16x8 q0, bf16x8 q1, bf16x8 q2, bf16x8 q3, const char* Kg, const unsigned short* Vg, __amdgpu_buffer_rsrc_t dsrd, unsigned dst, unsigned l) {
  const unsigned c = l & 15u, G = l >> 4;
  const f32x4 zero4 = {0.f, 0.f, 0.f, 0.f};
  float m = -3.0e38f;
#pragma nounroll
  for (int kt = 0; kt < 32; ++kt) {
    const char* kp = Kg + (16u * kt + c) * 256u + G * 16u;
    f32x4 a = mfma(*(const bf16x8*)kp, q0, zero4); a = mfma(*(const bf16x8*)(kp + 64), q1, a); a = mfma(*(const bf16x8*)(kp + 128), q2, a); a = mfma(*(const bf16x8*)(kp + 192), q3, a);
    m = fmaxf(fmaxf(m, fmaxf(a[0], a[1])), fmaxf(a[2], a[3]));
  }
  m = fmaxf(m, __shfl_xor(m, 16)); m = fmaxf(m, __shfl_xor(m, 32));
  f32x4 O[8];
#pragma unroll
  for (int u = 0; u < 8; ++u) O[u] = zero4;
  float ls = 0.f;
#pragma nounroll
  for (int ks = 0; ks < 16; ++ks) {
    unsigned pk[4];
#pragma unroll
    for (int hb = 0; hb < 2; ++hb) {
      const char* kp = Kg + (32u * ks + 16u * hb + c) * 256u + G * 16u;
      f32x4 a = mfma(*(const bf16x8*)kp, q0, zero4); a = mfma(*(const bf16x8*)(kp + 64), q1, a); a = mfma(*(const bf16x8*)(kp + 128), q2, a); a = mfma(*(const bf16x8*)(kp + 192), q3, a);
      const float e0 = __builtin_amdgcn_exp2f(a[0] - m), e1 = __builtin_amdgcn_exp2f(a[1] - m), e2 = __builtin_amdgcn_exp2f(a[2] - m), e3 = __builtin_amdgcn_exp2f(a[3] - m);
      ls += (e0 + e1) + (e2 + e3);
      pk[2 * hb] = cvtpk(e0, e1); pk[2 * hb + 1] = cvtpk(e2, e3);
    }
    const u32x4 pu = {pk[0], pk[1], pk[2], pk[3]};
    const unsigned short* vp0 = Vg + (32u * ks + 4u * G) * 128u + c; const unsigned short* vp1 = vp0 + 16 * 128;
#pragma unroll
    for (int u = 0; u < 8; ++u) {
      bf16x8 vf;
#pragma unroll
      for (int jj = 0; jj < 8; ++jj) vf[jj] = (short)(jj < 4 ? vp0 : vp1)[(jj & 3) * 128 + 16 * u];
      O[u] = mfma(vf, __builtin_bit_cast(bf16x8, pu), O[u]);
    }
  }
  ls += __shfl_xor(ls, 16); ls += __shfl_xor(ls, 32);
  const float inv = 1.0f / ls;
#pragma unroll
  for (int s = 0; s < 4; ++s) {
    u32x4 f = {cvtpk(O[2 * s][0] * inv, O[2 * s][1] * inv), cvtpk(O[2 * s][2] * inv, O[2 * s][3] * inv),
               cvtpk(O[2 * s + 1][0] * inv, O[2 * s + 1][1] * inv), cvtpk(O[2 * s + 1][2] * inv, O[2 * s + 1][3] * inv)};
    __builtin_amdgcn_raw_buffer_store_b128(f, dsrd, dst + s * 1024u + l * 16u, 0, 16);
  }
}

__global__ __launch_bounds__(512, 2) void k1_attn(const unsigned short* __restrict__ x, const unsigned short* __restrict__ kc,
                                                  const unsigned short* __restrict__ vc, const unsigned short* __restrict__ wq,
                                                  const unsigned short* __restrict__ wo, unsigned short* __restrict__ out, char* ws) {
  extern __shared__ __attribute__((aligned(16))) char lds[];
  TS(0);
  const unsigned lb = (unsigned)(size_t)lds;
  const unsigned tid = threadIdx.x, l = tid & 63u, c = l & 15u, G = l >> 4, q4 = (l >> 2) & 3u, p = l & 3u;
  const unsigned w = rfl(tid >> 6);
  const unsigned blk = blockIdx.x, g = blk & 7u, r_ = blk >> 3, j = r_ & 3u, b = r_ >> 2, h = 4u * g + j;
  const unsigned rot = ROT ? (b + 2u * j) % 9u : 0u;

  asm volatile("" : "+s"(x), "+s"(kc), "+s"(vc), "+s"(wq), "+s"(wo), "+s"(ws));
  const __amdgpu_buffer_rsrc_t srdF = __builtin_amdgcn_make_buffer_rsrc((void*)(ws + WS_FLAG), (short)0, 256 * 256, 0x00020000);
  const __amdgpu_buffer_rsrc_t srdOX = __builtin_amdgcn_make_buffer_rsrc((void*)(ws + WS_OX + (size_t)(b * NH + 4u * g) * 36864u), (short)0, 4 * 36864, 0x00020000);
  const __amdgpu_buffer_rsrc_t srdPX = __builtin_amdgcn_make_buffer_rsrc((void*)(ws + WS_PX + (size_t)(b * 4u + j) * 8u * PXB), (short)0, 8 * PXB, 0x00020000);
  unsigned finit, fadr;
  const __amdgpu_buffer_rsrc_t srdX = __builtin_amdgcn_make_buffer_rsrc((void*)(x + (size_t)b * SQ * DM), (short)0, SQ * DM * 2, 0x00020000);
  const __amdgpu_buffer_rsrc_t srdW = __builtin_amdgcn_make_buffer_rsrc((void*)(wq + (size_t)h * DH), (short)0, (DM * 4096 - h * DH) * 2, 0x00020000);
  const __amdgpu_buffer_rsrc_t srdK = __builtin_amdgcn_make_buffer_rsrc((void*)(kc + (size_t)(b * NH + h) * SK * DH), (short)0, SK * DH * 2, 0x00020000);
  const __amdgpu_buffer_rsrc_t srdV = __builtin_amdgcn_make_buffer_rsrc((void*)(vc + (size_t)(b * NH + h) * SK * DH), (short)0, SK * DH * 2, 0x00020000);

  const unsigned vx0 = (16u * w + (l >> 3)) * 1152u + (((l & 7u) ^ (G & 7u)) << 4);
  const unsigned vx1 = (16u * w + 8u + (l >> 3)) * 1152u + (((l & 7u) ^ ((4u + G) & 7u)) << 4);
  const unsigned vx9 = (128u + 2u * w + (l >> 5)) * 1152u + (((((l & 31u) >> 2) ^ w) & 7u) << 4) + ((l & 3u) << 2);
  const unsigned vw = (8u * w + G) * 8192u + ((((c >> 1) ^ (G | ((w & 1u) << 2)))) << 5) + ((c & 1u) << 4);
  const unsigned vkl = w * 2048u + l * 16u;
  const unsigned kw0 = lb + (8u * w + G) * 256u + ((c ^ (8u * (w & 1u) + G)) << 4);
  const unsigned kw1 = lb + (8u * w + 4u + G) * 256u + ((c ^ (8u * (w & 1u) + 4u + G)) << 4);
  const unsigned vv0 = (8u * w + G) * 256u + (((c >> 1) ^ G) << 5) + ((c & 1u) << 4);
  const unsigned vv1 = (8u * w + 4u + G) * 256u + (((c >> 1) ^ (4u + G)) << 5) + ((c & 1u) << 4);
  constexpr int KS[8] = {2, 3, 6, 2, 3, 6, 2, 3}, VS[8] = {7, 0, 1, 4, 5, 6, 2, 3};

  auto issueXW = [&](int i) { const unsigned s = lb + ((2 * i) & 7) * SLOT; unsigned kt = i + rot; kt = kt >= 9u ? kt - 9u : kt;
    dma16(s + w * 2048u, vx0, srdX, kt * 128u); dma16(s + w * 2048u + 1024u, vx1, srdX, kt * 128u); dma4(s + 16384u + w * 256u, vx9, srdX, kt * 128u);
    dma16(s + SLOT + w * 2048u, vw, srdW, kt * (64u * 8192u)); dma16(s + SLOT + w * 2048u + 1024u, vw, srdW, kt * (64u * 8192u) + 4u * 8192u); };
  u32x4 kp[8][2];
  auto loadK = [&](int t) {
    if (KV_NT) asm volatile("buffer_load_dwordx4 %0, %2, %3, %4 offen nt\n\tbuffer_load_dwordx4 %1, %2, %3, %4 offen offset:1024 nt" : "=&v"(kp[t][0]), "=&v"(kp[t][1]) : "v"(vkl), "s"(srdK), "s"(t * 16384) : "memory");
    else asm volatile("buffer_load_dwordx4 %0, %2, %3, %4 offen\n\tbuffer_load_dwordx4 %1, %2, %3, %4 offen offset:1024" : "=&v"(kp[t][0]), "=&v"(kp[t][1]) : "v"(vkl), "s"(srdK), "s"(t * 16384) : "memory");
  };
  auto writeK = [&](int t) {
    *(LAS u32x4*)(size_t)(kw0 + KS[t] * SLOT) = kp[t][0]; *(LAS u32x4*)(size_t)(kw1 + KS[t] * SLOT) = kp[t][1]; };
#define KWAIT(N, t) asm volatile("s_waitcnt vmcnt(%2)" : "+v"(kp[t][0]), "+v"(kp[t][1]) : "i"(N) : "memory")
  auto issueV = [&](int t) { const unsigned s = lb + VS[t] * SLOT + w * 2048u;
    dmakv(s, vv0, srdV, t * 16384); dmakv(s + 1024u, vv1, srdV, t * 16384); };

  constexpr int WS[8] = {7, 0, 1, 4, 2, 3, 5, 6};
  const __amdgpu_buffer_rsrc_t srdO = __builtin_amdgcn_make_buffer_rsrc((void*)(wo + (size_t)g * 512 * DM + j * 144), (short)0, (4096 * DM - g * 512 * DM - j * 144) * 2, 0x00020000);
  auto issueWO = [&](int i) {
    const unsigned ch0 = (2u * w) * 64u + l, ch1 = ch0 + 64u, dw = 4096u + w * 64u + l;
    const unsigned so = (((j + (i >> 1)) & 3u) * 2u + (i & 1)) * (64u * 1152u), s = lb + WS[i] * SLOT;
    dma16(s + w * 2048u, (ch0 / 18u) * 1152u + (ch0 % 18u) * 16u, srdO, so); dma16(s + w * 2048u + 1024u, (ch1 / 18u) * 1152u + (ch1 % 18u) * 16u, srdO, so);
    dma4(s + 16384u + w * 256u, (dw / 72u) * 1152u + (dw % 72u) * 4u, srdO, so); };
  auto issueWO6 = [&](int i) {
    const unsigned so = (((j + (i >> 1)) & 3u) * 2u + (i & 1)) * (64u * 1152u), s = lb + WS[i] * SLOT + (w - 1u) * 3072u;
#pragma unroll
    for (int e = 0; e < 3; ++e) { const unsigned ch = ((w - 1u) * 3u + e) * 64u + l; dma16(s + e * 1024u, (ch / 18u) * 1152u + (ch % 18u) * 16u, srdO, so); } };

  asm volatile("s_nop 4");
  issueXW(0); issueXW(1); issueXW(2); issueXW(3);

  const unsigned XCH = lb + 3u * SLOT + 16384u;
  const unsigned qg = w & 3u, hf = w >> 2, wv = 2u * qg + hf;
  const f32x4 zero4 = {0.f, 0.f, 0.f, 0.f};
  {
    f32x4 qa[2][4], qa9 = zero4;
#pragma unroll
    for (int t = 0; t < 4; ++t) { qa[0][t] = zero4; qa[1][t] = zero4; }
    const unsigned axo = lb + (32u * qg + c) * 128u + ((G ^ (c >> 1)) << 4);
    const unsigned ax9 = lb + (128u + c) * 128u + ((G ^ (c >> 1)) << 4);
    const unsigned awb = (8u * G + q4) * 256u + (((4u * hf + qg) ^ (q4 | ((G & 1u) << 2))) << 5) + 8u * p;
    bf16x8 af[2][4], bx[2][3];
    unsigned wb[2][4], xb[2][2], x9b[2][2];
#pragma unroll
    for (int hh = 0; hh < 2; ++hh) {
#pragma unroll
      for (int t = 0; t < 4; ++t) wb[hh][t] = opq(lb + (awb ^ (t << 5)) + hh * 4 * SLOT);
#pragma unroll
      for (int ks = 0; ks < 2; ++ks) { xb[hh][ks] = opq((axo ^ (ks << 6)) + hh * 4 * SLOT); x9b[hh][ks] = opq((ax9 ^ (ks << 6)) + hh * 4 * SLOT); }
    }
    auto rdA = [&](int gg, int t) { const int i = gg >> 1, ks = gg & 1, sl = (2 * i + 1) & 7;
      const unsigned a = wb[sl >> 2][t] + (sl & 3) * SLOT + ks * 8192;
      af[gg & 1][t] = cat(ldstr(a), ldstr(a + 1024)); };
    auto rdB = [&](int gg, int e) { const int i = gg >> 1, ks = gg & 1, sl = (2 * i) & 7;
      if (e < 2) bx[gg & 1][e] = lds128(xb[sl >> 2][ks] + (sl & 3) * SLOT + e * 2048); else bx[gg & 1][2] = lds128(x9b[sl >> 2][ks] + (sl & 3) * SLOT); };
    auto grp = [&](int gg) {
#pragma unroll
      for (int t = 0; t < 4; ++t) {
        qa[0][t] = mfma(af[gg & 1][t], bx[gg & 1][0], qa[0][t]);
        qa[1][t] = mfma(af[gg & 1][t], bx[gg & 1][1], qa[1][t]);
        if (t == 0) { qa9 = mfma(af[gg & 1][0], bx[gg & 1][2], qa9); PIN(qa9); }
        if (gg < 17) { if (t < 3) rdB(gg + 1, t); rdA(gg + 1, t); }
        SB();
      }
    };
    wait_bar<15>(); TS(1);
    rdB(0, 0); rdB(0, 1); rdB(0, 2);
#pragma unroll
    for (int t = 0; t < 4; ++t) rdA(0, t);
    SB();
#define LK(sg) do { _Pragma("unroll") for (int t_ = 0; t_ < 8; ++t_) if (SC.kseg[t_] == (sg)) loadK(t_); } while (0)
#define STAG 1
#define SEG(sg, ga, gb, XWI) do { if (STAG && hf == 0u) { if (XWI) issueXW(sg + 2); LK(sg); } \
      grp(ga); if (STAG == 2 && hf != 0u) { if (XWI) issueXW(sg + 2); LK(sg); } grp(gb); \
      if (STAG == 0 || (STAG == 1 && hf != 0u)) { if (XWI) issueXW(sg + 2); LK(sg); } } while (0)
    grp(0);  LK(0); wait_bar<cum(0) - posXW(1)>(); TS(2);
    SEG(1, 1, 2, 0);   wait_bar<cum(1) - posXW(2)>(); TS(3);
    SEG(2, 3, 4, 1);   wait_bar<cum(2) - posXW(3)>(); TS(4);
    SEG(3, 5, 6, 1);   wait_bar<cum(3) - posXW(4)>(); TS(5);
    SEG(4, 7, 8, 1);   wait_bar<cum(4) - posXW(5)>(); TS(6);
    SEG(5, 9, 10, 1);  wait_bar<cum(5) - posXW(6)>(); TS(7);
    SEG(6, 11, 12, 1); wait_bar<cum(6) - posXW(7)>(); TS(8);
    SEG(7, 13, 14, 0); wait_bar<cum(7) - posXW(8)>(); TS(9);
    SEG(8, 15, 16, 0); grp(17);
#undef SEG
#undef LK
    {
      const unsigned bj = (b * 4u + (l & 3u)) * 8u + g, bg = (b * 4u + j) * 8u + (l & 7u);
      unsigned a = blk * 256u;
      a = l < 40u ? blk * 256u + 128u + (l & 7u) * 4u : a;
      a = l < 32u ? blk * 256u : a;
      a = l < 28u ? blk * 256u + 64u + (l & 3u) * 4u : a;
      a = l < 24u ? bg * 256u + 128u + g * 4u : a;
      a = l < 16u ? bg * 256u + 32u : a;
      a = l < 8u ? bj * 256u + 64u + j * 4u : a;
      a = l < 4u ? bj * 256u : a;
      fadr = a;
    }
    asm volatile("buffer_load_dword %0, %1, %2, 0 offen sc1" : "=v"(finit) : "v"(fadr), "s"(srdF) : "memory");
    KWAIT(cl63(cum(8) + 1 - posK(0)), 0); writeK(0); KWAIT(cl63(cum(8) + 1 - posK(1)), 1); writeK(1);
    const unsigned QB = lb + 4u * SLOT;
#pragma unroll
    for (int e = 0; e < 2; ++e)
#pragma unroll
      for (int t = 0; t < 4; ++t) {
        const unsigned dt = 4u * hf + (t ^ qg);
        u32x2 v = {cvtpk(qa[e][t][0] * SL2E, qa[e][t][1] * SL2E), cvtpk(qa[e][t][2] * SL2E, qa[e][t][3] * SL2E)};
        *(LAS u32x2*)(size_t)(QB + (32u * qg + 16u * e + c) * 256u + (((2u * dt + (G >> 1)) ^ c) << 4) + (G & 1u) * 8u) = v;
      }
    { const unsigned dt = 4u * hf + qg;
      u32x2 v = {cvtpk(qa9[0] * SL2E, qa9[1] * SL2E), cvtpk(qa9[2] * SL2E, qa9[3] * SL2E)};
      *(LAS u32x2*)(size_t)(lb + MISC + c * 256u + (((2u * dt + (G >> 1)) ^ c) << 4) + (G & 1u) * 8u) = v; }
  }

  u32x4 P[8][2];
  float fsc[2], inv9;
  f32x4 mst;
  unsigned slow;
  u32x4 bown[4];
  {
    f32x4 lsum[2] = {zero4, zero4};
    bf16x8 qf[2][4], q9f[4], kf[3][4];
    f32x4 S[2][2][2];
    f32x4 ncv[2] = {zero4, zero4}, nc9 = zero4, S9f = zero4;
    float l9 = 0.f;
    const u32x4 onesu = {0x3F803F80u, 0x3F803F80u, 0x3F803F80u, 0x3F803F80u};
    const bf16x8 ones = __builtin_bit_cast(bf16x8, onesu);
    const unsigned tpar = (qg >> 1) ^ hf;
    const unsigned dutymask = (0x1111u << (2u * tpar + (qg & 1u)));
    const unsigned p9t = opq(lb + MISC + hf * 1024u + l * 16u + (qg & 1u) * 8u + tpar * 2048u);
    const unsigned akb = lb + c * 256u + ((G ^ c) << 4) + hf * 8192u;
    unsigned kb[2][4];
#pragma unroll
    for (int hh = 0; hh < 2; ++hh)
#pragma unroll
      for (int s = 0; s < 4; ++s) kb[hh][s] = opq((akb ^ (s << 6)) + hh * 4 * SLOT);
    auto rdK = [&](int un) { const int sl = KS[un >> 1];
#pragma unroll
      for (int s = 0; s < 4; ++s) kf[un % 3][s] = lds128(kb[sl >> 2][s] + (sl & 3) * SLOT + (un & 1) * 4096); };
    auto rowmax = [&](float m) {
      { auto r = __builtin_amdgcn_permlane32_swap(__float_as_uint(m), __float_as_uint(m), false, false); m = fmaxf(__uint_as_float(r[0]), __uint_as_float(r[1])); }
      { auto r = __builtin_amdgcn_permlane16_swap(__float_as_uint(m), __float_as_uint(m), false, false); m = fmaxf(__uint_as_float(r[0]), __uint_as_float(r[1])); }
      return m; };
    auto expack = [&](int T, int e) {
      const f32x4 a = S[T & 1][0][e], bq = S[T & 1][1][e];
      float e0 = __builtin_amdgcn_exp2f(a[0]), e1 = __builtin_amdgcn_exp2f(a[1]), e2 = __builtin_amdgcn_exp2f(a[2]), e3 = __builtin_amdgcn_exp2f(a[3]);
      float e4 = __builtin_amdgcn_exp2f(bq[0]), e5 = __builtin_amdgcn_exp2f(bq[1]), e6 = __builtin_amdgcn_exp2f(bq[2]), e7 = __builtin_amdgcn_exp2f(bq[3]);
      u32x4 f = {cvtpk(e0, e1), cvtpk(e2, e3), cvtpk(e4, e5), cvtpk(e6, e7)};
      P[T][e] = f;
      lsum[e] = mfma(ones, __builtin_bit_cast(bf16x8, f), lsum[e]);
      PIN(P[T][e]); PIN(lsum[e]);
    };
    auto exp9 = [&](f32x4 s, unsigned dst) {
      const float e0 = __builtin_amdgcn_exp2f(s[0]), e1 = __builtin_amdgcn_exp2f(s[1]), e2 = __builtin_amdgcn_exp2f(s[2]), e3 = __builtin_amdgcn_exp2f(s[3]);
      l9 += (e0 + e1) + (e2 + e3);
      u32x2 v = {cvtpk(e0, e1), cvtpk(e2, e3)};
      *(LAS u32x2*)(size_t)dst = v; };
    auto setref = [&]() {
#pragma unroll
      for (int e = 0; e < 2; ++e) {
        f32x4 (&Sp)[2][2] = S[0];
        float d = fmaxf(fmaxf(fmaxf(Sp[0][e][0], Sp[0][e][1]), fmaxf(Sp[0][e][2], Sp[0][e][3])), fmaxf(fmaxf(Sp[1][e][0], Sp[1][e][1]), fmaxf(Sp[1][e][2], Sp[1][e][3])));
        d = rowmax(d);
        Sp[0][e] -= d; Sp[1][e] -= d; ncv[e] -= d;
      }
      SB();
    };
    auto kunit = [&](int un) {
      const int t = un >> 1, i = un & 1;
      if (un + 2 < 16) rdK(un + 2);
      SB();
      f32x4 a0 = ncv[0], a1 = ncv[1];
#pragma unroll
      for (int s = 0; s < 4; ++s) { a0 = mfma(kf[un % 3][s], qf[0][s], a0); a1 = mfma(kf[un % 3][s], qf[1][s], a1); }
      S[t & 1][i][0] = a0; S[t & 1][i][1] = a1;
      if (t > 0) expack(t - 1, i);
      if (un == 8 || un == 12) exp9(S9f, p9t + (un / 2 - 2) * 2048u);
      SB();
      if (dutymask & (1u << un)) {
        f32x4 acc9 = nc9;
#pragma unroll
        for (int s = 0; s < 4; ++s) acc9 = mfma(kf[un % 3][s], q9f[s], acc9);
        S9f = acc9;
      }
      SB();
      if (un == 3) {
        const float m = rowmax(fmaxf(fmaxf(S9f[0], S9f[1]), fmaxf(S9f[2], S9f[3])));
        if (G == 0) *(LAS float*)(size_t)(XCH + w * 64u + c * 4u) = m;
        SB();
      }
      if (un == 1) setref();
    };
    lds_bar(); TS(10);
    {
      const unsigned qb = lb + 4u * SLOT + c * 256u;
#pragma unroll
      for (int s = 0; s < 4; ++s) {
        const unsigned o = (((4u * s + G) ^ c) << 4);
        qf[0][s] = lds128(qb + (2u * qg + hf) * 4096u + o);
        qf[1][s] = lds128(qb + (2u * qg + (1u - hf)) * 4096u + o);
        q9f[s] = lds128(lb + MISC + c * 256u + o);
      }
    }
    rdK(0); rdK(1);
    KWAIT(cl63(cum(8) + 1 - posK(2)), 2); writeK(2); SB();
    kunit(0); kunit(1); issueV(0); issueV(1); issueV(2);
    lds_bar(); TS(11); KWAIT(cl63(cum(8) + 7 - posK(3)), 3); writeK(3); kunit(2); kunit(3); issueV(3); issueV(4);
    lds_bar(); TS(12); KWAIT(cl63(cum(8) + 11 - posK(4)), 4); writeK(4);
    {
      float m = -3.0e38f;
#pragma unroll
      for (int i = 0; i < 8; ++i) m = fmaxf(m, *(const LAS float*)(size_t)(XCH + i * 64u + c * 4u));
      nc9 = (f32x4){-m, -m, -m, -m};
      exp9(S9f + nc9, p9t);
      SB();
    }
    kunit(4); kunit(5);
    lds_bar4(); TS(13); KWAIT(cl63(cum(8) + 11 - posK(5)), 5); writeK(5); kunit(6); kunit(7);
    lds_bar4(); TS(14); KWAIT(cl63(cum(8) + 11 - posK(6)), 6); writeK(6); kunit(8); kunit(9);
    lds_bar4(); TS(15); KWAIT(cl63(cum(8) + 11 - posK(7)), 7); writeK(7); kunit(10); kunit(11);
    lds_bar4(); TS(16); kunit(12); kunit(13); issueV(5);
    wait_bar<63>(); TS(17); kunit(14); kunit(15); issueV(6);
    expack(7, 0); expack(7, 1); exp9(S9f, p9t + 6 * 2048u);
    l9 += __shfl_xor(l9, 16); l9 += __shfl_xor(l9, 32);
    if (G == 0) *(LAS float*)(size_t)(XCH + 512u + w * 64u + c * 4u) = l9;
    {
      const bool bad = __any(!(lsum[0][0] <= THRP) || !(lsum[1][0] <= THRP) || !(l9 <= THRP));
      if (l == 0) *(LAS unsigned*)(size_t)(XCH + 1024u + w * 4u) = bad ? 1u : 0u; }
    if (G == 0) { const f32x4 st = {ncv[0][0], lsum[0][0], ncv[1][0], lsum[1][0]}; *(LAS f32x4*)(size_t)(lb + SLOT + 16384u + w * 256u + c * 16u) = st; }
    asm volatile("s_waitcnt vmcnt(12) lgkmcnt(0)\n\ts_barrier" : "+v"(finit) :: "memory"); TS(18);
    issueV(7);
    mst = (f32x4){ncv[0][0], lsum[0][0], ncv[1][0], lsum[1][0]};
    {
      const unsigned fl = *(const LAS unsigned*)(size_t)(XCH + 1024u + (l & 7u) * 4u);
      slow = rfl(__any(fl != 0u) ? 1u : 0u);
      if (__builtin_expect(slow != 0u, 0)) {
        const char* Kg = (const char*)(kc + (size_t)(b * NH + h) * SK * DH); const unsigned short* Vg = vc + (size_t)(b * NH + h) * SK * DH;
#pragma nounroll
        for (unsigned rep = 0; rep < (w == 0 ? 2u : 1u); ++rep)
          slow_tile_impl(rep ? q9f[0] : qf[0][0], rep ? q9f[1] : qf[0][1], rep ? q9f[2] : qf[0][2], rep ? q9f[3] : qf[0][3], Kg, Vg, srdOX, j * 36864u + (rep ? 8u : wv) * 4096u, l);
        asm volatile("s_waitcnt vmcnt(0) lgkmcnt(0)\n\ts_barrier" ::: "memory");
#pragma unroll
        for (int s = 0; s < 4; ++s) bown[s] = __builtin_amdgcn_raw_buffer_load_b128(srdOX, j * 36864u + wv * 4096u + ((s ^ (wv >> 1)) << 10) + l * 16u, 0, 16);
        issueWO(0); issueWO(1); issueWO(2); issueWO(3);
        asm volatile("s_waitcnt vmcnt(0)\n\ts_barrier" ::: "memory");
      }
    }
  }
  SB();

  if (slow == 0u) {
  f32x4 O[2][8], O9[2] = {zero4, zero4};
#pragma unroll
  for (int u = 0; u < 8; ++u) { O[0][u] = zero4; O[1][u] = zero4; }
  {
    constexpr int PD = 6;
    const unsigned avb = (32u * hf + 4u * G + q4) * 256u + ((wv ^ (4u * (G & 1u) + q4)) << 5) + 8u * p;
    bf16x8 vf[8], p9[2];
    unsigned vb[2][8];
#pragma unroll
    for (int hh = 0; hh < 2; ++hh)
#pragma unroll
      for (int u = 0; u < 8; ++u) vb[hh][u] = opq(lb + (avb ^ (u << 5)) + hh * 4 * SLOT);
    const unsigned p9b = opq(lb + MISC + hf * 1024u + l * 16u);
    auto rdV = [&](int f) { const int t = f >> 3, u = f & 7, sl = VS[t];
      const unsigned a = vb[sl >> 2][u] + (sl & 3) * SLOT;
      vf[f & 7] = cat(ldstr(a), ldstr(a + 4096));
      if (u == 0) p9[t & 1] = lds128(p9b + t * 2048u); };
    auto vfr = [&](int f) { const int t = f >> 3, u = f & 7;
      O[0][u] = mfma(vf[f & 7], __builtin_bit_cast(bf16x8, P[t][0]), O[0][u]);
      O[1][u] = mfma(vf[f & 7], __builtin_bit_cast(bf16x8, P[t][1]), O[1][u]);
      if (u < 2) { O9[u] = mfma(vf[f & 7], p9[t & 1], O9[u]); PIN(O9[u]); }
      if (f + PD < 64) rdV(f + PD);
      SB(); };
#pragma unroll
    for (int f = 0; f < PD; ++f) rdV(f);
    SB(); TS(19);
#define VT(t, N, ts) vfr(8 * t); vfr(8 * t + 1); wait_bar<N>(); TS(ts); vfr(8 * t + 2); vfr(8 * t + 3); vfr(8 * t + 4); vfr(8 * t + 5); vfr(8 * t + 6); vfr(8 * t + 7);
    VT(0, 12, 20)
    {
      const f32x4 st = *(const LAS f32x4*)(size_t)(lb + SLOT + 16384u + (w ^ 4u) * 256u + c * 16u);
#pragma unroll
      for (int e = 0; e < 2; ++e) {
        const float na = mst[2 * e], nb = st[2 * (1 - e)], nm = fminf(na, nb);
        const float fa = __builtin_amdgcn_exp2f(nm - na), fb = __builtin_amdgcn_exp2f(nm - nb);
        fsc[e] = fa * __builtin_amdgcn_rcpf(mst[2 * e + 1] * fa + st[2 * (1 - e) + 1] * fb);
      }
      float l9t = 0.f;
#pragma unroll
      for (int i = 0; i < 8; ++i) l9t += *(const LAS float*)(size_t)(XCH + 512u + i * 64u + c * 4u);
      inv9 = __builtin_amdgcn_rcpf(l9t);
      PIN(fsc[0]); PIN(fsc[1]); PIN(inv9);
    }
    VT(1, 10, 21) issueWO(0); VT(2, 11, 22) issueWO(1); VT(3, 12, 23) issueWO(2); VT(4, 13, 24) issueWO(3); VT(5, 14, 25) VT(6, 12, 26)
#undef VT
#pragma unroll
    for (int f = 56; f < 64; ++f) vfr(f);
  }
  TS(27);

  {
    const unsigned mine = lb + 5u * SLOT + w * 4608u;
#pragma unroll
    for (int u = 0; u < 8; ++u) { const f32x4 o = O[1][u] * fsc[1]; const u32x2 v = {cvtpk(o[0], o[1]), cvtpk(o[2], o[3])}; *(LAS u32x2*)(size_t)(mine + ((u ^ wv) << 9) + l * 8u) = v; }
    { const f32x4 o = O9[1] * inv9; const u32x2 v = {cvtpk(o[0], o[1]), cvtpk(o[2], o[3])}; *(LAS u32x2*)(size_t)(mine + 4096u + l * 8u) = v; }
  }
  asm volatile("s_waitcnt vmcnt(0) lgkmcnt(0)\n\ts_barrier" ::: "memory");
  if (w >= 1u && w <= 6u) { issueWO6(4); issueWO6(5); }
  {
    const unsigned oth = lb + 5u * SLOT + (w ^ 4u) * 4608u;
    auto up = [&](u32x2 v) { return (f32x4){__uint_as_float(v[0] << 16), __uint_as_float(v[0] & 0xffff0000u), __uint_as_float(v[1] << 16), __uint_as_float(v[1] & 0xffff0000u)}; };
#pragma unroll
    for (int u = 0; u < 8; ++u) O[0][u] = O[0][u] * fsc[0] + up(*(const LAS u32x2*)(size_t)(oth + ((u ^ wv) << 9) + l * 8u));
    O9[0] = O9[0] * inv9 + up(*(const LAS u32x2*)(size_t)(oth + 4096u + l * 8u));
  }
  const bool odd = (wv & 1u) != 0;
#pragma unroll
  for (int s = 0; s < 4; ++s) {
    unsigned a0 = cvtpk(O[0][2 * s][0], O[0][2 * s][1]), a1 = cvtpk(O[0][2 * s][2], O[0][2 * s][3]);
    unsigned b0 = cvtpk(O[0][2 * s + 1][0], O[0][2 * s + 1][1]), b1 = cvtpk(O[0][2 * s + 1][2], O[0][2 * s + 1][3]);
    bown[s] = odd ? (u32x4){b0, b1, a0, a1} : (u32x4){a0, a1, b0, b1};
    __builtin_amdgcn_raw_buffer_store_b128(bown[s], srdOX, j * 36864u + wv * 4096u + ((s ^ (wv >> 1)) << 10) + l * 16u, 0, 16);
  }
  { u32x2 v = {cvtpk(O9[0][0], O9[0][1]), cvtpk(O9[0][2], O9[0][3])};
    __builtin_amdgcn_raw_buffer_store_b64(v, srdOX, j * 36864u + 8u * 4096u + ((wv >> 1) << 10) + l * 16u + (wv & 1u) * 8u, 0, 16); }
  } else {
    if (w >= 1u && w <= 6u) { issueWO6(4); issueWO6(5); }
  }
  TS(28);

  unsigned hb = 1u;
  auto poll = [&](int stage) {
    const unsigned long long self1 = (1ull << j) | (1ull << (4u + j)), self2 = (1ull << (8u + g)) | (1ull << (16u + g));
    for (unsigned it = 0; it < 400000u; ++it) {
      unsigned v;
      asm volatile("buffer_load_dword %0, %1, %2, 0 offen sc1\n\ts_waitcnt vmcnt(0)" : "=v"(v) : "v"(fadr), "s"(srdF) : "memory");
      const unsigned long long ch = __ballot(v != finit) | self1 | self2;
      const bool ack1 = (ch & 0xF0ull) == 0xF0ull, ack2 = (ch & 0xFF0000ull) == 0xFF0000ull;
      if (stage == 1 && (ch & 0xFull) == 0xFull) break;
      if (stage == 2 && (ch & 0xFF00ull) == 0xFF00ull) break;
      if (stage == 3 && ack1 && ack2) break;
      if ((it & 63u) == 63u) {
        ++hb;
        if ((l == j && !ack1) || (stage >= 2 && l == 8u + g && !ack2)) __builtin_amdgcn_raw_buffer_store_b32(finit + hb, srdF, fadr, 0, 16);
      }
      __builtin_amdgcn_s_sleep(1);
    }
  };
  {
    f32x4 acc[9];
#pragma unroll
    for (int u = 0; u < 9; ++u) acc[u] = zero4;
    const unsigned aob = lb + (4u * G + q4) * 288u + 8u * p;
    {
      bf16x8 af[2][9];
      unsigned ao[4];
#pragma unroll
      for (int s = 0; s < 4; ++s) { const unsigned ks = s ^ (wv >> 1); ao[s] = opq(aob + ((ks >> 1) ? WS[1] : WS[0]) * SLOT + (ks & 1u) * 32u * 288u); }
      auto rdA = [&](int s, int u) { const unsigned a = ao[s] + u * 32u; af[s & 1][u] = cat(ldstr(a), ldstr(a + 16u * 288u)); };
#pragma unroll
      for (int u = 0; u < 9; ++u) rdA(0, u);
      SB();
#pragma unroll
      for (int s = 0; s < 4; ++s)
#pragma unroll
        for (int u = 0; u < 9; ++u) {
          acc[u] = mfma(af[s & 1][u], __builtin_bit_cast(bf16x8, bown[s]), acc[u]);
          if (s < 3) rdA(s + 1, u);
          SB();
        }
    }
    asm volatile("s_waitcnt vmcnt(0) lgkmcnt(0)\n\ts_barrier" ::: "memory");
    TS(29);
    if (w == 0) {
      if (l == j) __builtin_amdgcn_raw_buffer_store_b32(finit + 1u, srdF, fadr, 0, 16);
      poll(1);
      if (l >= 24u && l < 28u) __builtin_amdgcn_raw_buffer_store_b32(finit + 1u, srdF, fadr, 0, 16);
    } else if (w <= 6u) { issueWO6(6); issueWO6(7); }
    asm volatile("s_waitcnt vmcnt(0)\n\ts_barrier" ::: "memory");
    TS(30);
    bf16x8 bo[12], b9[16];
#pragma unroll
    for (int i = 0; i < 12; ++i) bo[i] = __builtin_bit_cast(bf16x8, __builtin_amdgcn_raw_buffer_load_b128(srdOX, wv * 4096u + l * 16u, ((j + 1u + (i >> 2)) & 3u) * 36864u + (i & 3) * 1024u, 16));
#pragma unroll
    for (int i = 0; i < 16; ++i) b9[i] = __builtin_bit_cast(bf16x8, __builtin_amdgcn_raw_buffer_load_b128(srdOX, 8u * 4096u + l * 16u, ((j + (i >> 2)) & 3u) * 36864u + (i & 3) * 1024u, 16));
    unsigned ab[2], a9[2]; ab[0] = opq(aob); ab[1] = opq(aob + 4u * SLOT); a9[0] = opq(aob + w * 32u); a9[1] = opq(aob + w * 32u + 4u * SLOT);
    bf16x8 ar[8];
    f32x4 acc9 = zero4, acc99 = zero4;
    auto pk = [&](f32x4 a, f32x4 bb) { return (u32x4){cvtpk(a[0], a[1]), cvtpk(a[2], a[3]), cvtpk(bb[0], bb[1]), cvtpk(bb[2], bb[3])}; };
    auto rdI = [&](int it) {
      int ks, u = 0, nine = 0;
      if (it < 96) { ks = 4 + ((it % 24) >> 1); u = 2 * (it / 24) + (it & 1); } else if (it < 108) { ks = 4 + (it - 96); u = 8; } else if (it < 124) { ks = it - 108; nine = 1; } else { ks = it - 124; u = 8; }
      const int sl = WS[ks >> 1];
      const unsigned a = (nine ? a9[sl >> 2] : ab[sl >> 2]) + (sl & 3) * SLOT + (ks & 1) * 32u * 288u + u * 32u;
      ar[it & 7] = cat(ldstr(a), ldstr(a + 16u * 288u)); };
    auto doI = [&](int it, int last) {
      if (it < 96) { const int kk = (it % 24) >> 1, u = 2 * (it / 24) + (it & 1); acc[u] = mfma(ar[it & 7], bo[kk], acc[u]); }
      else if (it < 108) acc[8] = mfma(ar[it & 7], bo[it - 96], acc[8]);
      else if (it < 124) acc9 = mfma(ar[it & 7], b9[it - 108], acc9);
      else acc99 = mfma(ar[it & 7], b9[it - 124], acc99);
      if (it + 6 < last) rdI(it + 6);
      if (it < 108 && it >= 28 && (it % 24) == 4) { const int pr = it / 24 - 1; __builtin_amdgcn_raw_buffer_store_b128(pk(acc[2 * pr], acc[2 * pr + 1]), srdPX, g * PXB + (w * 5u + pr) * 1024u + l * 16u, 0, 16); }
      SB(); };
#pragma unroll
    for (int it = 0; it < 6; ++it) rdI(it);
    SB();
#define D4(i) doI(i, 124); doI(i + 1, 124); doI(i + 2, 124); doI(i + 3, 124);
#define D12(i) D4(i) D4(i + 4) D4(i + 8)
    D12(0) D12(12) D12(24) D12(36) D12(48) D12(60) D12(72) D12(84) D12(96) D12(108) D4(120)
#undef D12
#undef D4
    __builtin_amdgcn_raw_buffer_store_b128(pk(acc[8], acc9), srdPX, g * PXB + (w * 5u + 4u) * 1024u + l * 16u, 0, 16);
    if (w == 7u) {
#pragma unroll
      for (int it = 124; it < 130; ++it) rdI(it);
#pragma unroll
      for (int it = 124; it < 140; ++it) doI(it, 140);
      __builtin_amdgcn_raw_buffer_store_b128(pk(acc99, acc99), srdPX, g * PXB + 40u * 1024u + l * 16u, 0, 16);
    }
  }
  TS(31);
  asm volatile("s_waitcnt vmcnt(0)\n\ts_barrier" ::: "memory");
  TS(32);
  if (w == 0) {
    if (l == 8u + g) __builtin_amdgcn_raw_buffer_store_b32(finit + 1u, srdF, fadr, 0, 16);
    poll(2);
    if (l >= 32u && l < 40u) __builtin_amdgcn_raw_buffer_store_b32(finit + 1u, srdF, fadr, 0, 16);
  }
  asm volatile("s_barrier" ::: "memory");
  TS(33);
  if ((w >= 1u && w <= 5u) || (w == 6u && g == 0u)) {
    const unsigned un = w <= 5u ? g * 5u + (w - 1u) : 40u;
    u32x4 pv[8];
#pragma unroll
    for (int gg = 0; gg < 8; ++gg) pv[gg] = __builtin_amdgcn_raw_buffer_load_b128(srdPX, un * 1024u + l * 16u, gg * PXB, 16);
    float sm[8];
#pragma unroll
    for (int k = 0; k < 4; ++k) {
      float lo = 0.f, hi = 0.f;
#pragma unroll
      for (int gg = 0; gg < 8; ++gg) { lo += __uint_as_float(pv[gg][k] << 16); hi += __uint_as_float(pv[gg][k] & 0xffff0000u); }
      sm[2 * k] = lo; sm[2 * k + 1] = hi;
    }
    const unsigned qsrc = 2u * (g & 3u) + (g >> 2);
    const unsigned pi = w - 1u;
    const unsigned qa = w <= 5u ? qsrc : 8u, na = w <= 5u ? 2u * pi : 8u;
    const unsigned qb = pi < 4u ? qsrc : 8u, nb = pi < 4u ? 2u * pi + 1u : g;
    const u32x2 va = {cvtpk(sm[0], sm[1]), cvtpk(sm[2], sm[3])}, vb = {cvtpk(sm[4], sm[5]), cvtpk(sm[6], sm[7])};
    *(u32x2*)(out + (size_t)(b * SQ + qa * 16u + c) * DM + j * 144u + na * 16u + G * 4u) = va;
    if (w <= 5u) *(u32x2*)(out + (size_t)(b * SQ + qb * 16u + c) * DM + j * 144u + nb * 16u + G * 4u) = vb;
  }
  TS(34);
  if (w == 0) poll(3);
}

extern "C" void kernel_launch(void* const* d_in, const int* in_sizes, int n_in, void* d_out, int out_size, void* d_ws, size_t ws_size, hipStream_t stream) {
  const unsigned short* x = (const unsigned short*)d_in[0];
  const unsigned short* kc = (const unsigned short*)d_in[1];
  const unsigned short* vc = (const unsigned short*)d_in[2];
  const unsigned short* wq = (const unsigned short*)d_in[3];
  const unsigned short* wo = (const unsigned short*)d_in[4];
  (void)hipFuncSetAttribute((const void*)k1_attn, hipFuncAttributeMaxDynamicSharedMemorySize, LDS_BYTES);
  k1_attn<<<256, 512, LDS_BYTES, stream>>>(x, kc, vc, wq, wo, (unsigned short*)d_out, (char*)d_ws);
}
```

```cpp
#include <hip/hip_runtime.h>
#include <hip/hip_bf16.h>

#define LAS __attribute__((address_space(3)))
#define DI __device__ __forceinline__
typedef short bf16x8 __attribute__((ext_vector_type(8)));
typedef short s16x4 __attribute__((ext_vector_type(4)));
typedef float f32x4 __attribute__((ext_vector_type(4)));
typedef float f32x2 __attribute__((ext_vector_type(2)));
typedef __bf16 bf16x2 __attribute__((ext_vector_type(2)));
typedef unsigned u32x4 __attribute__((ext_vector_type(4)));
typedef unsigned u32x2 __attribute__((ext_vector_type(2)));

constexpr int SQ = 144, DM = 576, NH = 32, SK = 512, DH = 128;
constexpr unsigned SLOT = 18432, MISC = 8 * SLOT, LDS_BYTES = MISC + 16384;
#define THRP 1.8446744073709552e19f
constexpr unsigned PXB = 41 * 1024;
constexpr size_t WS_OX = 0, WS_PX = (size_t)16 << 20, WS_FLAG = (size_t)64 << 20;
constexpr float SL2E = 0.08838834764831845f * 1.4426950408889634f;

DI unsigned rfl(unsigned x) { return __builtin_amdgcn_readfirstlane(x); }
DI unsigned cvtpk(float lo, float hi) { f32x2 v = {lo, hi}; bf16x2 b = __builtin_convertvector(v, bf16x2); return __builtin_bit_cast(unsigned, b); }
DI bf16x8 lds128(unsigned a) { return *(const LAS bf16x8*)(size_t)a; }
DI s16x4 ldstr(unsigned a) { return __builtin_amdgcn_ds_read_tr16_b64_v4i16((LAS s16x4*)(size_t)a); }
DI bf16x8 cat(s16x4 lo, s16x4 hi) { return __builtin_shufflevector(lo, hi, 0, 1, 2, 3, 4, 5, 6, 7); }
DI f32x4 mfma(bf16x8 a, bf16x8 b, f32x4 c) { return __builtin_amdgcn_mfma_f32_16x16x32_bf16(a, b, c, 0, 0, 0); }
#define SB() __builtin_amdgcn_sched_barrier(0)
DI unsigned opq(unsigned x) { asm volatile("" : "+v"(x)); return x; }
#define PIN(x) asm volatile("" : "+v"(x))
DI float max3f(float a, float b, float c) { float r; asm("v_max3_f32 %0, %1, %2, %3" : "=v"(r) : "v"(a), "v"(b), "v"(c)); return r; }
DI float max2f(float a, float b) { float r; asm("v_max_f32_e32 %0, %1, %2" : "=v"(r) : "v"(a), "v"(b)); return r; }

DI void dma16(unsigned ldsdst, unsigned voff, __amdgpu_buffer_rsrc_t srd, unsigned soff) {
  asm volatile("s_mov_b32 m0, %0\n\ts_nop 0\n\tbuffer_load_dwordx4 %1, %2, %3 offen lds" :: "s"(ldsdst), "v"(voff), "s"(srd), "s"(soff) : "memory");
}
DI void dma16nt(unsigned ldsdst, unsigned voff, __amdgpu_buffer_rsrc_t srd, unsigned soff) {
  asm volatile("s_mov_b32 m0, %0\n\ts_nop 0\n\tbuffer_load_dwordx4 %1, %2, %3 offen nt lds" :: "s"(ldsdst), "v"(voff), "s"(srd), "s"(soff) : "memory");
}
DI void dma4(unsigned ldsdst, unsigned voff, __amdgpu_buffer_rsrc_t srd, unsigned soff) {
  asm volatile("s_mov_b32 m0, %0\n\ts_nop 0\n\tbuffer_load_dword %1, %2, %3 offen lds" :: "s"(ldsdst), "v"(voff), "s"(srd), "s"(soff) : "memory");
}
template <int N> DI void wait_bar() { asm volatile("s_waitcnt vmcnt(%0)\n\ts_barrier" :: "i"(N) : "memory"); }
template <int N> DI void wait_bar_l() { asm volatile("s_waitcnt vmcnt(%0) lgkmcnt(0)\n\ts_barrier" :: "i"(N) : "memory"); }
DI void lds_bar4() { asm volatile("s_waitcnt lgkmcnt(4)\n\ts_barrier" ::: "memory"); }
DI void lds_bar() { asm volatile("s_waitcnt lgkmcnt(0)\n\ts_barrier" ::: "memory"); }

#define TS(i) do{}while(0)
#define PRETOUCH 1
#define ROT 1
#define KV_NT 1
DI void dmakv(unsigned ldsdst, unsigned voff, __amdgpu_buffer_rsrc_t srd, unsigned soff) {
  if (KV_NT) dma16nt(ldsdst, voff, srd, soff); else dma16(ldsdst, voff, srd, soff);
}

#define KSEG {1, 3, 5, 6, 7, 7, 8, 8}
struct Sched { int kseg[8]; };
constexpr Sched SC = {KSEG};
constexpr int nK(int s) { int n = 0; for (int t = 0; t < 8; ++t) n += SC.kseg[t] == s; return n; }
constexpr int cum(int s) { int n = 20; for (int i = 0; i <= s; ++i) n += ((i >= 2 && i <= 6) ? 5 : 0) + 2 * nK(i); return n; }
constexpr int posXW(int n) { return n <= 3 ? 5 * (n + 1) : cum(n - 3) + 5; }
constexpr int posK(int t) { int s = SC.kseg[t], n = cum(s - 1) + ((s >= 2 && s <= 6) ? 5 : 0); for (int i = 0; i <= t; ++i) n += (SC.kseg[i] == s) ? 2 : 0; return n; }
constexpr int cl63(int n) { return n > 63 ? 63 : n; }

DI void slow_tile_impl(bf16x8 q0, bf16x8 q1, bf16x8 q2, bf16x8 q3, const char* Kg, const unsigned short* Vg, __amdgpu_buffer_rsrc_t dsrd, unsigned dst, unsigned l) {
  const unsigned c = l & 15u, G = l >> 4;
  const f32x4 zero4 = {0.f, 0.f, 0.f, 0.f};
  float m = -3.0e38f;
#pragma nounroll
  for (int kt = 0; kt < 32; ++kt) {
    const char* kp = Kg + (16u * kt + c) * 256u + G * 16u;
    f32x4 a = mfma(*(const bf16x8*)kp, q0, zero4); a = mfma(*(const bf16x8*)(kp + 64), q1, a); a = mfma(*(const bf16x8*)(kp + 128), q2, a); a = mfma(*(const bf16x8*)(kp + 192), q3, a);
    m = fmaxf(fmaxf(m, fmaxf(a[0], a[1])), fmaxf(a[2], a[3]));
  }
  m = fmaxf(m, __shfl_xor(m, 16)); m = fmaxf(m, __shfl_xor(m, 32));
  f32x4 O[8];
#pragma unroll
  for (int u = 0; u < 8; ++u) O[u] = zero4;
  float ls = 0.f;
#pragma nounroll
  for (int ks = 0; ks < 16; ++ks) {
    unsigned pk[4];
#pragma unroll
    for (int hb = 0; hb < 2; ++hb) {
      const char* kp = Kg + (32u * ks + 16u * hb + c) * 256u + G * 16u;
      f32x4 a = mfma(*(const bf16x8*)kp, q0, zero4); a = mfma(*(const bf16x8*)(kp + 64), q1, a); a = mfma(*(const bf16x8*)(kp + 128), q2, a); a = mfma(*(const bf16x8*)(kp + 192), q3, a);
      const float e0 = __builtin_amdgcn_exp2f(a[0] - m), e1 = __builtin_amdgcn_exp2f(a[1] - m), e2 = __builtin_amdgcn_exp2f(a[2] - m), e3 = __builtin_amdgcn_exp2f(a[3] - m);
      ls += (e0 + e1) + (e2 + e3);
      pk[2 * hb] = cvtpk(e0, e1); pk[2 * hb + 1] = cvtpk(e2, e3);
    }
    const u32x4 pu = {pk[0], pk[1], pk[2], pk[3]};
    const unsigned short* vp0 = Vg + (32u * ks + 4u * G) * 128u + c; const unsigned short* vp1 = vp0 + 16 * 128;
#pragma unroll
    for (int u = 0; u < 8; ++u) {
      bf16x8 vf;
#pragma unroll
      for (int jj = 0; jj < 8; ++jj) vf[jj] = (short)(jj < 4 ? vp0 : vp1)[(jj & 3) * 128 + 16 * u];
      O[u] = mfma(vf, __builtin_bit_cast(bf16x8, pu), O[u]);
    }
  }
  ls += __shfl_xor(ls, 16); ls += __shfl_xor(ls, 32);
  const float inv = 1.0f / ls;
#pragma unroll
  for (int s = 0; s < 4; ++s) {
    u32x4 f = {cvtpk(O[2 * s][0] * inv, O[2 * s][1] * inv), cvtpk(O[2 * s][2] * inv, O[2 * s][3] * inv),
               cvtpk(O[2 * s + 1][0] * inv, O[2 * s + 1][1] * inv), cvtpk(O[2 * s + 1][2] * inv, O[2 * s + 1][3] * inv)};
    __builtin_amdgcn_raw_buffer_store_b128(f, dsrd, dst + s * 1024u + l * 16u, 0, 16);
  }
}

__global__ __launch_bounds__(512, 2) void k1_attn(const unsigned short* __restrict__ x, const unsigned short* __restrict__ kc,
                                                  const unsigned short* __restrict__ vc, const unsigned short* __restrict__ wq,
                                                  const unsigned short* __restrict__ wo, unsigned short* __restrict__ out, char* ws) {
  extern __shared__ __attribute__((aligned(16))) char lds[];
  TS(0);
  const unsigned lb = (unsigned)(size_t)lds;
  const unsigned tid = threadIdx.x, l = tid & 63u, c = l & 15u, G = l >> 4, q4 = (l >> 2) & 3u, p = l & 3u;
  const unsigned w = rfl(tid >> 6);
  const unsigned blk = blockIdx.x, g = blk & 7u, r_ = blk >> 3, j = r_ & 3u, b = r_ >> 2, h = 4u * g + j;
  const unsigned rot = ROT ? (b + 2u * j) % 9u : 0u;

  asm volatile("" : "+s"(x), "+s"(kc), "+s"(vc), "+s"(wq), "+s"(wo), "+s"(ws));
  const __amdgpu_buffer_rsrc_t srdF = __builtin_amdgcn_make_buffer_rsrc((void*)(ws + WS_FLAG), (short)0, 256 * 256, 0x00020000);
  const __amdgpu_buffer_rsrc_t srdOX = __builtin_amdgcn_make_buffer_rsrc((void*)(ws + WS_OX + (size_t)(b * NH + 4u * g) * 36864u), (short)0, 4 * 36864, 0x00020000);
  const __amdgpu_buffer_rsrc_t srdPX = __builtin_amdgcn_make_buffer_rsrc((void*)(ws + WS_PX + (size_t)(b * 4u + j) * 8u * PXB), (short)0, 8 * PXB, 0x00020000);
  unsigned finit, fadr;
  const __amdgpu_buffer_rsrc_t srdX = __builtin_amdgcn_make_buffer_rsrc((void*)(x + (size_t)b * SQ * DM), (short)0, SQ * DM * 2, 0x00020000);
  const __amdgpu_buffer_rsrc_t srdW = __builtin_amdgcn_make_buffer_rsrc((void*)(wq + (size_t)h * DH), (short)0, (DM * 4096 - h * DH) * 2, 0x00020000);
  const __amdgpu_buffer_rsrc_t srdK = __builtin_amdgcn_make_buffer_rsrc((void*)(kc + (size_t)(b * NH + h) * SK * DH), (short)0, SK * DH * 2, 0x00020000);
  const __amdgpu_buffer_rsrc_t srdV = __builtin_amdgcn_make_buffer_rsrc((void*)(vc + (size_t)(b * NH + h) * SK * DH), (short)0, SK * DH * 2, 0x00020000);

  const unsigned vx0 = (16u * w + (l >> 3)) * 1152u + (((l & 7u) ^ (G & 7u)) << 4);
  const unsigned vx1 = (16u * w + 8u + (l >> 3)) * 1152u + (((l & 7u) ^ ((4u + G) & 7u)) << 4);
  const unsigned vx9 = (128u + 2u * w + (l >> 5)) * 1152u + (((((l & 31u) >> 2) ^ w) & 7u) << 4) + ((l & 3u) << 2);
  const unsigned vw = (8u * w + G) * 8192u + ((((c >> 1) ^ (G | ((w & 1u) << 2)))) << 5) + ((c & 1u) << 4);
  const unsigned vkl = w * 2048u + l * 16u;
  const unsigned kw0 = lb + (8u * w + G) * 256u + ((c ^ (8u * (w & 1u) + G)) << 4);
  const unsigned kw1 = lb + (8u * w + 4u + G) * 256u + ((c ^ (8u * (w & 1u) + 4u + G)) << 4);
  const unsigned vv0 = (8u * w + G) * 256u + (((c >> 1) ^ G) << 5) + ((c & 1u) << 4);
  const unsigned vv1 = (8u * w + 4u + G) * 256u + (((c >> 1) ^ (4u + G)) << 5) + ((c & 1u) << 4);
  constexpr int KS[8] = {2, 3, 6, 2, 3, 6, 2, 3}, VS[8] = {7, 0, 1, 4, 5, 6, 2, 3};

  auto issueXW = [&](int i) { const unsigned s = lb + ((2 * i) & 7) * SLOT; unsigned kt = i + rot; kt = kt >= 9u ? kt - 9u : kt;
    dma16(s + w * 2048u, vx0, srdX, kt * 128u); dma16(s + w * 2048u + 1024u, vx1, srdX, kt * 128u); dma4(s + 16384u + w * 256u, vx9, srdX, kt * 128u);
    dma16(s + SLOT + w * 2048u, vw, srdW, kt * (64u * 8192u)); dma16(s + SLOT + w * 2048u + 1024u, vw, srdW, kt * (64u * 8192u) + 4u * 8192u); };
  u32x4 kp[8][2];
  auto loadK = [&](int t) {
    if (KV_NT) asm volatile("buffer_load_dwordx4 %0, %2, %3, %4 offen nt\n\tbuffer_load_dwordx4 %1, %2, %3, %4 offen offset:1024 nt" : "=&v"(kp[t][0]), "=&v"(kp[t][1]) : "v"(vkl), "s"(srdK), "s"(t * 16384) : "memory");
    else asm volatile("buffer_load_dwordx4 %0, %2, %3, %4 offen\n\tbuffer_load_dwordx4 %1, %2, %3, %4 offen offset:1024" : "=&v"(kp[t][0]), "=&v"(kp[t][1]) : "v"(vkl), "s"(srdK), "s"(t * 16384) : "memory");
  };
  auto writeK = [&](int t) {
    *(LAS u32x4*)(size_t)(kw0 + KS[t] * SLOT) = kp[t][0]; *(LAS u32x4*)(size_t)(kw1 + KS[t] * SLOT) = kp[t][1]; };
#define KWAIT(N, t) asm volatile("s_waitcnt vmcnt(%2)" : "+v"(kp[t][0]), "+v"(kp[t][1]) : "i"(N) : "memory")
  auto issueV = [&](int t) { const unsigned s = lb + VS[t] * SLOT + w * 2048u;
    dmakv(s, vv0, srdV, t * 16384); dmakv(s + 1024u, vv1, srdV, t * 16384); };

  constexpr int WS[8] = {7, 0, 1, 4, 2, 3, 5, 6};
  const __amdgpu_buffer_rsrc_t srdO = __builtin_amdgcn_make_buffer_rsrc((void*)(wo + (size_t)g * 512 * DM + j * 144), (short)0, (4096 * DM - g * 512 * DM - j * 144) * 2, 0x00020000);
  auto issueWO = [&](int i) {
    const unsigned ch0 = (2u * w) * 64u + l, ch1 = ch0 + 64u, dw = 4096u + w * 64u + l;
    const unsigned so = (((j + (i >> 1)) & 3u) * 2u + (i & 1)) * (64u * 1152u), s = lb + WS[i] * SLOT;
    dma16(s + w * 2048u, (ch0 / 18u) * 1152u + (ch0 % 18u) * 16u, srdO, so); dma16(s + w * 2048u + 1024u, (ch1 / 18u) * 1152u + (ch1 % 18u) * 16u, srdO, so);
    dma4(s + 16384u + w * 256u, (dw / 72u) * 1152u + (dw % 72u) * 4u, srdO, so); };
  auto issueWO6 = [&](int i) {
    const unsigned so = (((j + (i >> 1)) & 3u) * 2u + (i & 1)) * (64u * 1152u), s = lb + WS[i] * SLOT + (w - 1u) * 3072u;
#pragma unroll
    for (int e = 0; e < 3; ++e) { const unsigned ch = ((w - 1u) * 3u + e) * 64u + l; dma16(s + e * 1024u, (ch / 18u) * 1152u + (ch % 18u) * 16u, srdO, so); } };

  asm volatile("s_nop 4");
  issueXW(0); issueXW(1); issueXW(2); issueXW(3);
  unsigned pt0, pt1, pt2, pt3, pt4, pt5, pt6;
  {
    const char* a0 = ws + WS_FLAG + blk * 256u;
    const char* a1 = ws + WS_OX + (size_t)(b * NH + 4u * g) * 36864u;
    const char* a2 = ws + WS_PX + (size_t)(b * 4u + j) * 8u * PXB;
    const char* a3 = (const char*)(out + (size_t)b * SQ * DM);
    const char* a4 = (const char*)(wo + (size_t)g * 512 * DM + j * 144);
    asm volatile("s_load_dword %0, %7, 0x0\n\ts_load_dword %1, %8, 0x0\n\ts_load_dword %2, %8, 0x23ff0\n\ts_load_dword %3, %9, 0x0\n\ts_load_dword %4, %9, 0x51ff0\n\ts_load_dword %5, %10, 0x0\n\ts_load_dword %6, %11, 0x0"
                 : "=&s"(pt0), "=&s"(pt1), "=&s"(pt2), "=&s"(pt3), "=&s"(pt4), "=&s"(pt5), "=&s"(pt6) : "s"(a0), "s"(a1), "s"(a2), "s"(a3), "s"(a4) : "memory");
  }

  const unsigned XCH = lb + 3u * SLOT + 16384u;
  const unsigned qg = w & 3u, hf = w >> 2, wv = 2u * qg + hf;
  const f32x4 zero4 = {0.f, 0.f, 0.f, 0.f};
  {
    f32x4 qa[2][4], qa9 = zero4;
#pragma unroll
    for (int t = 0; t < 4; ++t) { qa[0][t] = zero4; qa[1][t] = zero4; }
    const unsigned axo = lb + (32u * qg + c) * 128u + ((G ^ (c >> 1)) << 4);
    const unsigned ax9 = lb + (128u + c) * 128u + ((G ^ (c >> 1)) << 4);
    const unsigned awb = (8u * G + q4) * 256u + (((4u * hf + qg) ^ (q4 | ((G & 1u) << 2))) << 5) + 8u * p;
    bf16x8 af[2][4], bx[2][3];
    unsigned wb[2][4], xb[2][2], x9b[2][2];
#pragma unroll
    for (int hh = 0; hh < 2; ++hh) {
#pragma unroll
      for (int t = 0; t < 4; ++t) wb[hh][t] = opq(lb + (awb ^ (t << 5)) + hh * 4 * SLOT);
#pragma unroll
      for (int ks = 0; ks < 2; ++ks) { xb[hh][ks] = opq((axo ^ (ks << 6)) + hh * 4 * SLOT); x9b[hh][ks] = opq((ax9 ^ (ks << 6)) + hh * 4 * SLOT); }
    }
    auto rdA = [&](int gg, int t) { const int i = gg >> 1, ks = gg & 1, sl = (2 * i + 1) & 7;
      const unsigned a = wb[sl >> 2][t] + (sl & 3) * SLOT + ks * 8192;
      af[gg & 1][t] = cat(ldstr(a), ldstr(a + 1024)); };
    auto rdB = [&](int gg, int e) { const int i = gg >> 1, ks = gg & 1, sl = (2 * i) & 7;
      if (e < 2) bx[gg & 1][e] = lds128(xb[sl >> 2][ks] + (sl & 3) * SLOT + e * 2048); else bx[gg & 1][2] = lds128(x9b[sl >> 2][ks] + (sl & 3) * SLOT); };
    auto grp = [&](int gg) {
#pragma unroll
      for (int t = 0; t < 4; ++t) {
        qa[0][t] = mfma(af[gg & 1][t], bx[gg & 1][0], qa[0][t]);
        qa[1][t] = mfma(af[gg & 1][t], bx[gg & 1][1], qa[1][t]);
        if (t == 0) { qa9 = mfma(af[gg & 1][0], bx[gg & 1][2], qa9); PIN(qa9); }
        if (gg < 17) { if (t < 3) rdB(gg + 1, t); rdA(gg + 1, t); }
        SB();
      }
    };
    asm volatile("s_waitcnt vmcnt(15) lgkmcnt(0)\n\ts_barrier" : "+s"(pt0), "+s"(pt1), "+s"(pt2), "+s"(pt3), "+s"(pt4), "+s"(pt5), "+s"(pt6) :: "memory"); TS(1);
    rdB(0, 0); rdB(0, 1); rdB(0, 2);
#pragma unroll
    for (int t = 0; t < 4; ++t) rdA(0, t);
    SB();
#define LK(sg) do { _Pragma("unroll") for (int t_ = 0; t_ < 8; ++t_) if (SC.kseg[t_] == (sg)) loadK(t_); } while (0)
#define STAG 1
#define SEG(sg, ga, gb, XWI) do { if (STAG && hf == 0u) { if (XWI) issueXW(sg + 2); LK(sg); } \
      grp(ga); if (STAG == 2 && hf != 0u) { if (XWI) issueXW(sg + 2); LK(sg); } grp(gb); \
      if (STAG == 0 || (STAG == 1 && hf != 0u)) { if (XWI) issueXW(sg + 2); LK(sg); } } while (0)
    grp(0);  LK(0); wait_bar<cum(0) - posXW(1)>(); TS(2);
    SEG(1, 1, 2, 0);   wait_bar<cum(1) - posXW(2)>(); TS(3);
    SEG(2, 3, 4, 1);   wait_bar<cum(2) - posXW(3)>(); TS(4);
    SEG(3, 5, 6, 1);   wait_bar<cum(3) - posXW(4)>(); TS(5);
    SEG(4, 7, 8, 1);   wait_bar<cum(4) - posXW(5)>(); TS(6);
    SEG(5, 9, 10, 1);  wait_bar<cum(5) - posXW(6)>(); TS(7);
    SEG(6, 11, 12, 1); wait_bar<cum(6) - posXW(7)>(); TS(8);
    SEG(7, 13, 14, 0); wait_bar<cum(7) - posXW(8)>(); TS(9);
    SEG(8, 15, 16, 0); grp(17);
#undef SEG
#undef LK
    {
      const unsigned bj = (b * 4u + (l & 3u)) * 8u + g, bg = (b * 4u + j) * 8u + (l & 7u);
      unsigned a = blk * 256u;
      a = l < 40u ? blk * 256u + 128u + (l & 7u) * 4u : a;
      a = l < 32u ? blk * 256u : a;
      a = l < 28u ? blk * 256u + 64u + (l & 3u) * 4u : a;
      a = l < 24u ? bg * 256u + 128u + g * 4u : a;
      a = l < 16u ? bg * 256u + 32u : a;
      a = l < 8u ? bj * 256u + 64u + j * 4u : a;
      a = l < 4u ? bj * 256u : a;
      fadr = a;
    }
    asm volatile("buffer_load_dword %0, %1, %2, 0 offen sc1" : "=v"(finit) : "v"(fadr), "s"(srdF) : "memory");
    KWAIT(cl63(cum(8) + 1 - posK(0)), 0); writeK(0); KWAIT(cl63(cum(8) + 1 - posK(1)), 1); writeK(1);
    const unsigned QB = lb + 4u * SLOT;
#pragma unroll
    for (int e = 0; e < 2; ++e)
#pragma unroll
      for (int t = 0; t < 4; ++t) {
        const unsigned dt = 4u * hf + (t ^ qg);
        u32x2 v = {cvtpk(qa[e][t][0] * SL2E, qa[e][t][1] * SL2E), cvtpk(qa[e][t][2] * SL2E, qa[e][t][3] * SL2E)};
        *(LAS u32x2*)(size_t)(QB + (32u * qg + 16u * e + c) * 256u + (((2u * dt + (G >> 1)) ^ c) << 4) + (G & 1u) * 8u) = v;
      }
    { const unsigned dt = 4u * hf + qg;
      u32x2 v = {cvtpk(qa9[0] * SL2E, qa9[1] * SL2E), cvtpk(qa9[2] * SL2E, qa9[3] * SL2E)};
      *(LAS u32x2*)(size_t)(lb + MISC + c * 256u + (((2u * dt + (G >> 1)) ^ c) << 4) + (G & 1u) * 8u) = v; }
  }

  u32x4 P[8][2];
  float fsc[2], inv9;
  f32x4 mst;
  unsigned slow;
  u32x4 bown[4];
  {
    f32x4 lsum[2] = {zero4, zero4};
    bf16x8 qf[2][4], q9f[4], kf[3][4];
    f32x4 S[2][2][2];
    f32x4 ncv[2] = {zero4, zero4}, nc9 = zero4, S9f = zero4;
    float l9 = 0.f;
    const u32x4 onesu = {0x3F803F80u, 0x3F803F80u, 0x3F803F80u, 0x3F803F80u};
    const bf16x8 ones = __builtin_bit_cast(bf16x8, onesu);
    const unsigned tpar = (qg >> 1) ^ hf;
    const unsigned dutymask = (0x1111u << (2u * tpar + (qg & 1u)));
    const unsigned p9t = opq(lb + MISC + hf * 1024u + l * 16u + (qg & 1u) * 8u + tpar * 2048u);
    const unsigned akb = lb + c * 256u + ((G ^ c) << 4) + hf * 8192u;
    unsigned kb[2][4];
#pragma unroll
    for (int hh = 0; hh < 2; ++hh)
#pragma unroll
      for (int s = 0; s < 4; ++s) kb[hh][s] = opq((akb ^ (s << 6)) + hh * 4 * SLOT);
    auto rdK = [&](int un) { const int sl = KS[un >> 1];
#pragma unroll
      for (int s = 0; s < 4; ++s) kf[un % 3][s] = lds128(kb[sl >> 2][s] + (sl & 3) * SLOT + (un & 1) * 4096); };
    auto rowmax = [&](float m) {
      { auto r = __builtin_amdgcn_permlane32_swap(__float_as_uint(m), __float_as_uint(m), false, false); m = fmaxf(__uint_as_float(r[0]), __uint_as_float(r[1])); }
      { auto r = __builtin_amdgcn_permlane16_swap(__float_as_uint(m), __float_as_uint(m), false, false); m = fmaxf(__uint_as_float(r[0]), __uint_as_float(r[1])); }
      return m; };
    auto expack = [&](int T, int e) {
      const f32x4 a = S[T & 1][0][e], bq = S[T & 1][1][e];
      float e0 = __builtin_amdgcn_exp2f(a[0]), e1 = __builtin_amdgcn_exp2f(a[1]), e2 = __builtin_amdgcn_exp2f(a[2]), e3 = __builtin_amdgcn_exp2f(a[3]);
      float e4 = __builtin_amdgcn_exp2f(bq[0]), e5 = __builtin_amdgcn_exp2f(bq[1]), e6 = __builtin_amdgcn_exp2f(bq[2]), e7 = __builtin_amdgcn_exp2f(bq[3]);
      u32x4 f = {cvtpk(e0, e1), cvtpk(e2, e3), cvtpk(e4, e5), cvtpk(e6, e7)};
      P[T][e] = f;
      lsum[e] = mfma(ones, __builtin_bit_cast(bf16x8, f), lsum[e]);
      PIN(P[T][e]); PIN(lsum[e]);
    };
    auto exp9 = [&](f32x4 s, unsigned dst) {
      const float e0 = __builtin_amdgcn_exp2f(s[0]), e1 = __builtin_amdgcn_exp2f(s[1]), e2 = __builtin_amdgcn_exp2f(s[2]), e3 = __builtin_amdgcn_exp2f(s[3]);
      l9 += (e0 + e1) + (e2 + e3);
      u32x2 v = {cvtpk(e0, e1), cvtpk(e2, e3)};
      *(LAS u32x2*)(size_t)dst = v; };
    auto setref = [&]() {
#pragma unroll
      for (int e = 0; e < 2; ++e) {
        f32x4 (&Sp)[2][2] = S[0];
        float d = fmaxf(fmaxf(fmaxf(Sp[0][e][0], Sp[0][e][1]), fmaxf(Sp[0][e][2], Sp[0][e][3])), fmaxf(fmaxf(Sp[1][e][0], Sp[1][e][1]), fmaxf(Sp[1][e][2], Sp[1][e][3])));
        d = rowmax(d);
        Sp[0][e] -= d; Sp[1][e] -= d; ncv[e] -= d;
      }
      SB();
    };
    auto kunit = [&](int un) {
      const int t = un >> 1, i = un & 1;
      if (un + 2 < 16) rdK(un + 2);
      SB();
      f32x4 a0 = ncv[0], a1 = ncv[1];
#pragma unroll
      for (int s = 0; s < 4; ++s) { a0 = mfma(kf[un % 3][s], qf[0][s], a0); a1 = mfma(kf[un % 3][s], qf[1][s], a1); }
      S[t & 1][i][0] = a0; S[t & 1][i][1] = a1;
      if (t > 0) expack(t - 1, i);
      if (un == 8 || un == 12) exp9(S9f, p9t + (un / 2 - 2) * 2048u);
      SB();
      if (dutymask & (1u << un)) {
        f32x4 acc9 = nc9;
#pragma unroll
        for (int s = 0; s < 4; ++s) acc9 = mfma(kf[un % 3][s], q9f[s], acc9);
        S9f = acc9;
      }
      SB();
      if (un == 3) {
        const float m = rowmax(fmaxf(fmaxf(S9f[0], S9f[1]), fmaxf(S9f[2], S9f[3])));
        if (G == 0) *(LAS float*)(size_t)(XCH + w * 64u + c * 4u) = m;
        SB();
      }
      if (un == 1) setref();
    };
    lds_bar(); TS(10);
    {
      const unsigned qb = lb + 4u * SLOT + c * 256u;
#pragma unroll
      for (int s = 0; s < 4; ++s) {
        const unsigned o = (((4u * s + G) ^ c) << 4);
        qf[0][s] = lds128(qb + (2u * qg + hf) * 4096u + o);
        qf[1][s] = lds128(qb + (2u * qg + (1u - hf)) * 4096u + o);
        q9f[s] = lds128(lb + MISC + c * 256u + o);
      }
    }
    rdK(0); rdK(1);
    KWAIT(cl63(cum(8) + 1 - posK(2)), 2); writeK(2); SB();
    kunit(0); kunit(1); issueV(0); issueV(1); issueV(2);
    lds_bar(); TS(11); KWAIT(cl63(cum(8) + 7 - posK(3)), 3); writeK(3); kunit(2); kunit(3); issueV(3); issueV(4);
    lds_bar(); TS(12); KWAIT(cl63(cum(8) + 11 - posK(4)), 4); writeK(4);
    {
      float m = -3.0e38f;
#pragma unroll
      for (int i = 0; i < 8; ++i) m = fmaxf(m, *(const LAS float*)(size_t)(XCH + i * 64u + c * 4u));
      nc9 = (f32x4){-m, -m, -m, -m};
      exp9(S9f + nc9, p9t);
      SB();
    }
    kunit(4); kunit(5);
    lds_bar4(); TS(13); KWAIT(cl63(cum(8) + 11 - posK(5)), 5); writeK(5); kunit(6); kunit(7);
    lds_bar4(); TS(14); KWAIT(cl63(cum(8) + 11 - posK(6)), 6); writeK(6); kunit(8); kunit(9);
    lds_bar4(); TS(15); KWAIT(cl63(cum(8) + 11 - posK(7)), 7); writeK(7); kunit(10); kunit(11);
    lds_bar4(); TS(16); kunit(12); kunit(13); issueV(5);
    wait_bar<63>(); TS(17); kunit(14); kunit(15); issueV(6);
    expack(7, 0); expack(7, 1); exp9(S9f, p9t + 6 * 2048u);
    l9 += __shfl_xor(l9, 16); l9 += __shfl_xor(l9, 32);
    if (G == 0) *(LAS float*)(size_t)(XCH + 512u + w * 64u + c * 4u) = l9;
    {
      const bool bad = __any(!(lsum[0][0] <= THRP) || !(lsum[1][0] <= THRP) || !(l9 <= THRP));
      if (l == 0) *(LAS unsigned*)(size_t)(XCH + 1024u + w * 4u) = bad ? 1u : 0u; }
    if (G == 0) { const f32x4 st = {ncv[0][0], lsum[0][0], ncv[1][0], lsum[1][0]}; *(LAS f32x4*)(size_t)(lb + SLOT + 16384u + w * 256u + c * 16u) = st; }
    asm volatile("s_waitcnt vmcnt(12) lgkmcnt(0)\n\ts_barrier" : "+v"(finit) :: "memory"); TS(18);
    issueV(7);
    mst = (f32x4){ncv[0][0], lsum[0][0], ncv[1][0], lsum[1][0]};
    {
      const unsigned fl = *(const LAS unsigned*)(size_t)(XCH + 1024u + (l & 7u) * 4u);
      slow = rfl(__any(fl != 0u) ? 1u : 0u);
      if (__builtin_expect(slow != 0u, 0)) {
        const char* Kg = (const char*)(kc + (size_t)(b * NH + h) * SK * DH); const unsigned short* Vg = vc + (size_t)(b * NH + h) * SK * DH;
#pragma nounroll
        for (unsigned rep = 0; rep < (w == 0 ? 2u : 1u); ++rep)
          slow_tile_impl(rep ? q9f[0] : qf[0][0], rep ? q9f[1] : qf[0][1], rep ? q9f[2] : qf[0][2], rep ? q9f[3] : qf[0][3], Kg, Vg, srdOX, j * 36864u + (rep ? 8u : wv) * 4096u, l);
        asm volatile("s_waitcnt vmcnt(0) lgkmcnt(0)\n\ts_barrier" ::: "memory");
#pragma unroll
        for (int s = 0; s < 4; ++s) bown[s] = __builtin_amdgcn_raw_buffer_load_b128(srdOX, j * 36864u + wv * 4096u + ((s ^ (wv >> 1)) << 10) + l * 16u, 0, 16);
        issueWO(0); issueWO(1); issueWO(2); issueWO(3);
        asm volatile("s_waitcnt vmcnt(0)\n\ts_barrier" ::: "memory");
      }
    }
  }
  SB();

  if (slow == 0u) {
  f32x4 O[2][8], O9[2] = {zero4, zero4};
#pragma unroll
  for (int u = 0; u < 8; ++u) { O[0][u] = zero4; O[1][u] = zero4; }
  {
    constexpr int PD = 6;
    const unsigned avb = (32u * hf + 4u * G + q4) * 256u + ((wv ^ (4u * (G & 1u) + q4)) << 5) + 8u * p;
    bf16x8 vf[8], p9[2];
    unsigned vb[2][8];
#pragma unroll
    for (int hh = 0; hh < 2; ++hh)
#pragma unroll
      for (int u = 0; u < 8; ++u) vb[hh][u] = opq(lb + (avb ^ (u << 5)) + hh * 4 * SLOT);
    const unsigned p9b = opq(lb + MISC + hf * 1024u + l * 16u);
    auto rdV = [&](int f) { const int t = f >> 3, u = f & 7, sl = VS[t];
      const unsigned a = vb[sl >> 2][u] + (sl & 3) * SLOT;
      vf[f & 7] = cat(ldstr(a), ldstr(a + 4096));
      if (u == 0) p9[t & 1] = lds128(p9b + t * 2048u); };
    auto vfr = [&](int f) { const int t = f >> 3, u = f & 7;
      O[0][u] = mfma(vf[f & 7], __builtin_bit_cast(bf16x8, P[t][0]), O[0][u]);
      O[1][u] = mfma(vf[f & 7], __builtin_bit_cast(bf16x8, P[t][1]), O[1][u]);
      if (u < 2) { O9[u] = mfma(vf[f & 7], p9[t & 1], O9[u]); PIN(O9[u]); }
      if (f + PD < 64) rdV(f + PD);
      SB(); };
#pragma unroll
    for (int f = 0; f < PD; ++f) rdV(f);
    SB(); TS(19);
#define VT(t, N, ts) vfr(8 * t); vfr(8 * t + 1); wait_bar<N>(); TS(ts); vfr(8 * t + 2); vfr(8 * t + 3); vfr(8 * t + 4); vfr(8 * t + 5); vfr(8 * t + 6); vfr(8 * t + 7);
    VT(0, 12, 20)
    {
      const f32x4 st = *(const LAS f32x4*)(size_t)(lb + SLOT + 16384u + (w ^ 4u) * 256u + c * 16u);
#pragma unroll
      for (int e = 0; e < 2; ++e) {
        const float na = mst[2 * e], nb = st[2 * (1 - e)], nm = fminf(na, nb);
        const float fa = __builtin_amdgcn_exp2f(nm - na), fb = __builtin_amdgcn_exp2f(nm - nb);
        fsc[e] = fa * __builtin_amdgcn_rcpf(mst[2 * e + 1] * fa + st[2 * (1 - e) + 1] * fb);
      }
      float l9t = 0.f;
#pragma unroll
      for (int i = 0; i < 8; ++i) l9t += *(const LAS float*)(size_t)(XCH + 512u + i * 64u + c * 4u);
      inv9 = __builtin_amdgcn_rcpf(l9t);
      PIN(fsc[0]); PIN(fsc[1]); PIN(inv9);
    }
    VT(1, 10, 21) issueWO(0); VT(2, 11, 22) issueWO(1); VT(3, 12, 23) issueWO(2); VT(4, 13, 24) issueWO(3); VT(5, 14, 25) VT(6, 12, 26)
#undef VT
#pragma unroll
    for (int f = 56; f < 64; ++f) vfr(f);
  }
  TS(27);

  {
    const unsigned mine = lb + 5u * SLOT + w * 4608u;
#pragma unroll
    for (int u = 0; u < 8; ++u) { const f32x4 o = O[1][u] * fsc[1]; const u32x2 v = {cvtpk(o[0], o[1]), cvtpk(o[2], o[3])}; *(LAS u32x2*)(size_t)(mine + ((u ^ wv) << 9) + l * 8u) = v; }
    { const f32x4 o = O9[1] * inv9; const u32x2 v = {cvtpk(o[0], o[1]), cvtpk(o[2], o[3])}; *(LAS u32x2*)(size_t)(mine + 4096u + l * 8u) = v; }
  }
  asm volatile("s_waitcnt vmcnt(0) lgkmcnt(0)\n\ts_barrier" ::: "memory");
  if (w >= 1u && w <= 6u) { issueWO6(4); issueWO6(5); }
  {
    const unsigned oth = lb + 5u * SLOT + (w ^ 4u) * 4608u;
    auto up = [&](u32x2 v) { return (f32x4){__uint_as_float(v[0] << 16), __uint_as_float(v[0] & 0xffff0000u), __uint_as_float(v[1] << 16), __uint_as_float(v[1] & 0xffff0000u)}; };
#pragma unroll
    for (int u = 0; u < 8; ++u) O[0][u] = O[0][u] * fsc[0] + up(*(const LAS u32x2*)(size_t)(oth + ((u ^ wv) << 9) + l * 8u));
    O9[0] = O9[0] * inv9 + up(*(const LAS u32x2*)(size_t)(oth + 4096u + l * 8u));
  }
  const bool odd = (wv & 1u) != 0;
#pragma unroll
  for (int s = 0; s < 4; ++s) {
    unsigned a0 = cvtpk(O[0][2 * s][0], O[0][2 * s][1]), a1 = cvtpk(O[0][2 * s][2], O[0][2 * s][3]);
    unsigned b0 = cvtpk(O[0][2 * s + 1][0], O[0][2 * s + 1][1]), b1 = cvtpk(O[0][2 * s + 1][2], O[0][2 * s + 1][3]);
    bown[s] = odd ? (u32x4){b0, b1, a0, a1} : (u32x4){a0, a1, b0, b1};
    __builtin_amdgcn_raw_buffer_store_b128(bown[s], srdOX, j * 36864u + wv * 4096u + ((s ^ (wv >> 1)) << 10) + l * 16u, 0, 16);
  }
  { u32x2 v = {cvtpk(O9[0][0], O9[0][1]), cvtpk(O9[0][2], O9[0][3])};
    __builtin_amdgcn_raw_buffer_store_b64(v, srdOX, j * 36864u + 8u * 4096u + ((wv >> 1) << 10) + l * 16u + (wv & 1u) * 8u, 0, 16); }
  } else {
    if (w >= 1u && w <= 6u) { issueWO6(4); issueWO6(5); }
  }
  TS(28);

  unsigned hb = 1u;
  auto poll = [&](int stage) {
    const unsigned long long self1 = (1ull << j) | (1ull << (4u + j)), self2 = (1ull << (8u + g)) | (1ull << (16u + g));
    for (unsigned it = 0; it < 400000u; ++it) {
      unsigned v;
      asm volatile("buffer_load_dword %0, %1, %2, 0 offen sc1\n\ts_waitcnt vmcnt(0)" : "=v"(v) : "v"(fadr), "s"(srdF) : "memory");
      const unsigned long long ch = __ballot(v != finit) | self1 | self2;
      const bool ack1 = (ch & 0xF0ull) == 0xF0ull, ack2 = (ch & 0xFF0000ull) == 0xFF0000ull;
      if (stage == 1 && (ch & 0xFull) == 0xFull) break;
      if (stage == 2 && (ch & 0xFF00ull) == 0xFF00ull) break;
      if (stage == 3 && ack1 && ack2) break;
      if ((it & 63u) == 63u) {
        ++hb;
        if ((l == j && !ack1) || (stage >= 2 && l == 8u + g && !ack2)) __builtin_amdgcn_raw_buffer_store_b32(finit + hb, srdF, fadr, 0, 16);
      }
      __builtin_amdgcn_s_sleep(1);
    }
  };
  {
    f32x4 acc[9];
#pragma unroll
    for (int u = 0; u < 9; ++u) acc[u] = zero4;
    const unsigned aob = lb + (4u * G + q4) * 288u + 8u * p;
    {
      bf16x8 af[2][9];
      unsigned ao[4];
#pragma unroll
      for (int s = 0; s < 4; ++s) { const unsigned ks = s ^ (wv >> 1); ao[s] = opq(aob + ((ks >> 1) ? WS[1] : WS[0]) * SLOT + (ks & 1u) * 32u * 288u); }
      auto rdA = [&](int s, int u) { const unsigned a = ao[s] + u * 32u; af[s & 1][u] = cat(ldstr(a), ldstr(a + 16u * 288u)); };
#pragma unroll
      for (int u = 0; u < 9; ++u) rdA(0, u);
      SB();
#pragma unroll
      for (int s = 0; s < 4; ++s)
#pragma unroll
        for (int u = 0; u < 9; ++u) {
          acc[u] = mfma(af[s & 1][u], __builtin_bit_cast(bf16x8, bown[s]), acc[u]);
          if (s < 3) rdA(s + 1, u);
          SB();
        }
    }
    asm volatile("s_waitcnt vmcnt(0) lgkmcnt(0)\n\ts_barrier" ::: "memory");
    TS(29);
    if (w == 0) {
      if (l == j) __builtin_amdgcn_raw_buffer_store_b32(finit + 1u, srdF, fadr, 0, 16);
      poll(1);
      if (l >= 24u && l < 28u) __builtin_amdgcn_raw_buffer_store_b32(finit + 1u, srdF, fadr, 0, 16);
    } else if (w <= 6u) { issueWO6(6); issueWO6(7); }
    asm volatile("s_waitcnt vmcnt(0)\n\ts_barrier" ::: "memory");
    TS(30);
    bf16x8 bo[12], b9[16];
#pragma unroll
    for (int i = 0; i < 12; ++i) bo[i] = __builtin_bit_cast(bf16x8, __builtin_amdgcn_raw_buffer_load_b128(srdOX, wv * 4096u + l * 16u, ((j + 1u + (i >> 2)) & 3u) * 36864u + (i & 3) * 1024u, 16));
#pragma unroll
    for (int i = 0; i < 16; ++i) b9[i] = __builtin_bit_cast(bf16x8, __builtin_amdgcn_raw_buffer_load_b128(srdOX, 8u * 4096u + l * 16u, ((j + (i >> 2)) & 3u) * 36864u + (i & 3) * 1024u, 16));
    unsigned ab[2], a9[2]; ab[0] = opq(aob); ab[1] = opq(aob + 4u * SLOT); a9[0] = opq(aob + w * 32u); a9[1] = opq(aob + w * 32u + 4u * SLOT);
    bf16x8 ar[8];
    f32x4 acc9 = zero4, acc99 = zero4;
    auto pk = [&](f32x4 a, f32x4 bb) { return (u32x4){cvtpk(a[0], a[1]), cvtpk(a[2], a[3]), cvtpk(bb[0], bb[1]), cvtpk(bb[2], bb[3])}; };
    auto rdI = [&](int it) {
      int ks, u = 0, nine = 0;
      if (it < 96) { ks = 4 + ((it % 24) >> 1); u = 2 * (it / 24) + (it & 1); } else if (it < 108) { ks = 4 + (it - 96); u = 8; } else if (it < 124) { ks = it - 108; nine = 1; } else { ks = it - 124; u = 8; }
      const int sl = WS[ks >> 1];
      const unsigned a = (nine ? a9[sl >> 2] : ab[sl >> 2]) + (sl & 3) * SLOT + (ks & 1) * 32u * 288u + u * 32u;
      ar[it & 7] = cat(ldstr(a), ldstr(a + 16u * 288u)); };
    auto doI = [&](int it, int last) {
      if (it < 96) { const int kk = (it % 24) >> 1, u = 2 * (it / 24) + (it & 1); acc[u] = mfma(ar[it & 7], bo[kk], acc[u]); }
      else if (it < 108) acc[8] = mfma(ar[it & 7], bo[it - 96], acc[8]);
      else if (it < 124) acc9 = mfma(ar[it & 7], b9[it - 108], acc9);
      else acc99 = mfma(ar[it & 7], b9[it - 124], acc99);
      if (it + 6 < last) rdI(it + 6);
      if (it < 108 && it >= 28 && (it % 24) == 4) { const int pr = it / 24 - 1; __builtin_amdgcn_raw_buffer_store_b128(pk(acc[2 * pr], acc[2 * pr + 1]), srdPX, g * PXB + (w * 5u + pr) * 1024u + l * 16u, 0, 16); }
      SB(); };
#pragma unroll
    for (int it = 0; it < 6; ++it) rdI(it);
    SB();
#define D4(i) doI(i, 124); doI(i + 1, 124); doI(i + 2, 124); doI(i + 3, 124);
#define D12(i) D4(i) D4(i + 4) D4(i + 8)
    D12(0) D12(12) D12(24) D12(36) D12(48) D12(60) D12(72) D12(84) D12(96) D12(108) D4(120)
#undef D12
#undef D4
    __builtin_amdgcn_raw_buffer_store_b128(pk(acc[8], acc9), srdPX, g * PXB + (w * 5u + 4u) * 1024u + l * 16u, 0, 16);
    if (w == 7u) {
#pragma unroll
      for (int it = 124; it < 130; ++it) rdI(it);
#pragma unroll
      for (int it = 124; it < 140; ++it) doI(it, 140);
      __builtin_amdgcn_raw_buffer_store_b128(pk(acc99, acc99), srdPX, g * PXB + 40u * 1024u + l * 16u, 0, 16);
    }
  }
  TS(31);
  asm volatile("s_waitcnt vmcnt(0)\n\ts_barrier" ::: "memory");
  TS(32);
  if (w == 0) {
    if (l == 8u + g) __builtin_amdgcn_raw_buffer_store_b32(finit + 1u, srdF, fadr, 0, 16);
    poll(2);
    if (l >= 32u && l < 40u) __builtin_amdgcn_raw_buffer_store_b32(finit + 1u, srdF, fadr, 0, 16);
  }
  asm volatile("s_barrier" ::: "memory");
  TS(33);
  if ((w >= 1u && w <= 5u) || (w == 6u && g == 0u)) {
    const unsigned un = w <= 5u ? g * 5u + (w - 1u) : 40u;
    u32x4 pv[8];
#pragma unroll
    for (int gg = 0; gg < 8; ++gg) pv[gg] = __builtin_amdgcn_raw_buffer_load_b128(srdPX, un * 1024u + l * 16u, gg * PXB, 16);
    float sm[8];
#pragma unroll
    for (int k = 0; k < 4; ++k) {
      float lo = 0.f, hi = 0.f;
#pragma unroll
      for (int gg = 0; gg < 8; ++gg) { lo += __uint_as_float(pv[gg][k] << 16); hi += __uint_as_float(pv[gg][k] & 0xffff0000u); }
      sm[2 * k] = lo; sm[2 * k + 1] = hi;
    }
    const unsigned qsrc = 2u * (g & 3u) + (g >> 2);
    const unsigned pi = w - 1u;
    const unsigned qa = w <= 5u ? qsrc : 8u, na = w <= 5u ? 2u * pi : 8u;
    const unsigned qb = pi < 4u ? qsrc : 8u, nb = pi < 4u ? 2u * pi + 1u : g;
    const u32x2 va = {cvtpk(sm[0], sm[1]), cvtpk(sm[2], sm[3])}, vb = {cvtpk(sm[4], sm[5]), cvtpk(sm[6], sm[7])};
    *(u32x2*)(out + (size_t)(b * SQ + qa * 16u + c) * DM + j * 144u + na * 16u + G * 4u) = va;
    if (w <= 5u) *(u32x2*)(out + (size_t)(b * SQ + qb * 16u + c) * DM + j * 144u + nb * 16u + G * 4u) = vb;
  }
  TS(34);
  if (w == 0) poll(3);
}

extern "C" void kernel_launch(void* const* d_in, const int* in_sizes, int n_in, void* d_out, int out_size, void* d_ws, size_t ws_size, hipStream_t stream) {
  const unsigned short* x = (const unsigned short*)d_in[0];
  const unsigned short* kc = (const unsigned short*)d_in[1];
  const unsigned short* vc = (const unsigned short*)d_in[2];
  const unsigned short* wq = (const unsigned short*)d_in[3];
  const unsigned short* wo = (const unsigned short*)d_in[4];
  (void)hipFuncSetAttribute((const void*)k1_attn, hipFuncAttributeMaxDynamicSharedMemorySize, LDS_BYTES);
  k1_attn<<<256, 512, LDS_BYTES, stream>>>(x, kc, vc, wq, wo, (unsigned short*)d_out, (char*)d_ws);
}
```

```cpp
#include <hip/hip_runtime.h>
#include <hip/hip_bf16.h>

#define LAS __attribute__((address_space(3)))
#define DI __device__ __forceinline__
typedef short bf16x8 __attribute__((ext_vector_type(8)));
typedef short s16x4 __attribute__((ext_vector_type(4)));
typedef float f32x4 __attribute__((ext_vector_type(4)));
typedef float f32x2 __attribute__((ext_vector_type(2)));
typedef __bf16 bf16x2 __attribute__((ext_vector_type(2)));
typedef unsigned u32x4 __attribute__((ext_vector_type(4)));
typedef unsigned u32x2 __attribute__((ext_vector_type(2)));

constexpr int SQ = 144, DM = 576, NH = 32, SK = 512, DH = 128;
constexpr unsigned SLOT = 18432, MISC = 8 * SLOT, LDS_BYTES = MISC + 16384;
#define THRP 1.8446744073709552e19f
constexpr unsigned PXB = 45 * 1024;
constexpr size_t WS_OX = 0, WS_PX = (size_t)16 << 20, WS_FLAG = (size_t)64 << 20;
constexpr float SL2E = 0.08838834764831845f * 1.4426950408889634f;

DI unsigned rfl(unsigned x) { return __builtin_amdgcn_readfirstlane(x); }
DI unsigned cvtpk(float lo, float hi) { f32x2 v = {lo, hi}; bf16x2 b = __builtin_convertvector(v, bf16x2); return __builtin_bit_cast(unsigned, b); }
DI bf16x8 lds128(unsigned a) { return *(const LAS bf16x8*)(size_t)a; }
DI s16x4 ldstr(unsigned a) { return __builtin_amdgcn_ds_read_tr16_b64_v4i16((LAS s16x4*)(size_t)a); }
DI bf16x8 cat(s16x4 lo, s16x4 hi) { return __builtin_shufflevector(lo, hi, 0, 1, 2, 3, 4, 5, 6, 7); }
DI f32x4 mfma(bf16x8 a, bf16x8 b, f32x4 c) { return __builtin_amdgcn_mfma_f32_16x16x32_bf16(a, b, c, 0, 0, 0); }
#define SB() __builtin_amdgcn_sched_barrier(0)
DI unsigned opq(unsigned x) { asm volatile("" : "+v"(x)); return x; }
#define PIN(x) asm volatile("" : "+v"(x))
DI float max3f(float a, float b, float c) { float r; asm("v_max3_f32 %0, %1, %2, %3" : "=v"(r) : "v"(a), "v"(b), "v"(c)); return r; }
DI float max2f(float a, float b) { float r; asm("v_max_f32_e32 %0, %1, %2" : "=v"(r) : "v"(a), "v"(b)); return r; }

DI void dma16(unsigned ldsdst, unsigned voff, __amdgpu_buffer_rsrc_t srd, unsigned soff) {
  asm volatile("s_mov_b32 m0, %0\n\ts_nop 0\n\tbuffer_load_dwordx4 %1, %2, %3 offen lds" :: "s"(ldsdst), "v"(voff), "s"(srd), "s"(soff) : "memory");
}
DI void dma16nt(unsigned ldsdst, unsigned voff, __amdgpu_buffer_rsrc_t srd, unsigned soff) {
  asm volatile("s_mov_b32 m0, %0\n\ts_nop 0\n\tbuffer_load_dwordx4 %1, %2, %3 offen nt lds" :: "s"(ldsdst), "v"(voff), "s"(srd), "s"(soff) : "memory");
}
DI void dma4(unsigned ldsdst, unsigned voff, __amdgpu_buffer_rsrc_t srd, unsigned soff) {
  asm volatile("s_mov_b32 m0, %0\n\ts_nop 0\n\tbuffer_load_dword %1, %2, %3 offen lds" :: "s"(ldsdst), "v"(voff), "s"(srd), "s"(soff) : "memory");
}
template <int N> DI void wait_bar() { asm volatile("s_waitcnt vmcnt(%0)\n\ts_barrier" :: "i"(N) : "memory"); }
template <int N> DI void wait_bar_l() { asm volatile("s_waitcnt vmcnt(%0) lgkmcnt(0)\n\ts_barrier" :: "i"(N) : "memory"); }
DI void lds_bar4() { asm volatile("s_waitcnt lgkmcnt(4)\n\ts_barrier" ::: "memory"); }
DI void lds_bar() { asm volatile("s_waitcnt lgkmcnt(0)\n\ts_barrier" ::: "memory"); }

#define TS(i) do{}while(0)
#define ROT 1
#define KV_NT 1
DI void dmakv(unsigned ldsdst, unsigned voff, __amdgpu_buffer_rsrc_t srd, unsigned soff) {
  if (KV_NT) dma16nt(ldsdst, voff, srd, soff); else dma16(ldsdst, voff, srd, soff);
}

#define KSEG {1, 3, 5, 6, 7, 7, 8, 8}
struct Sched { int kseg[8]; };
constexpr Sched SC = {KSEG};
constexpr int nK(int s) { int n = 0; for (int t = 0; t < 8; ++t) n += SC.kseg[t] == s; return n; }
constexpr int cum(int s) { int n = 20; for (int i = 0; i <= s; ++i) n += ((i >= 2 && i <= 6) ? 5 : 0) + 2 * nK(i); return n; }
constexpr int posXW(int n) { return n <= 3 ? 5 * (n + 1) : cum(n - 3) + 5; }
constexpr int posK(int t) { int s = SC.kseg[t], n = cum(s - 1) + ((s >= 2 && s <= 6) ? 5 : 0); for (int i = 0; i <= t; ++i) n += (SC.kseg[i] == s) ? 2 : 0; return n; }
constexpr int cl63(int n) { return n > 63 ? 63 : n; }

DI void slow_tile_impl(bf16x8 q0, bf16x8 q1, bf16x8 q2, bf16x8 q3, const char* Kg, const unsigned short* Vg, __amdgpu_buffer_rsrc_t dsrd, unsigned dst, unsigned l) {
  const unsigned c = l & 15u, G = l >> 4;
  const f32x4 zero4 = {0.f, 0.f, 0.f, 0.f};
  float m = -3.0e38f;
#pragma nounroll
  for (int kt = 0; kt < 32; ++kt) {
    const char* kp = Kg + (16u * kt + c) * 256u + G * 16u;
    f32x4 a = mfma(*(const bf16x8*)kp, q0, zero4); a = mfma(*(const bf16x8*)(kp + 64), q1, a); a = mfma(*(const bf16x8*)(kp + 128), q2, a); a = mfma(*(const bf16x8*)(kp + 192), q3, a);
    m = fmaxf(fmaxf(m, fmaxf(a[0], a[1])), fmaxf(a[2], a[3]));
  }
  m = fmaxf(m, __shfl_xor(m, 16)); m = fmaxf(m, __shfl_xor(m, 32));
  f32x4 O[8];
#pragma unroll
  for (int u = 0; u < 8; ++u) O[u] = zero4;
  float ls = 0.f;
#pragma nounroll
  for (int ks = 0; ks < 16; ++ks) {
    unsigned pk[4];
#pragma unroll
    for (int hb = 0; hb < 2; ++hb) {
      const char* kp = Kg + (32u * ks + 16u * hb + c) * 256u + G * 16u;
      f32x4 a = mfma(*(const bf16x8*)kp, q0, zero4); a = mfma(*(const bf16x8*)(kp + 64), q1, a); a = mfma(*(const bf16x8*)(kp + 128), q2, a); a = mfma(*(const bf16x8*)(kp + 192), q3, a);
      const float e0 = __builtin_amdgcn_exp2f(a[0] - m), e1 = __builtin_amdgcn_exp2f(a[1] - m), e2 = __builtin_amdgcn_exp2f(a[2] - m), e3 = __builtin_amdgcn_exp2f(a[3] - m);
      ls += (e0 + e1) + (e2 + e3);
      pk[2 * hb] = cvtpk(e0, e1); pk[2 * hb + 1] = cvtpk(e2, e3);
    }
    const u32x4 pu = {pk[0], pk[1], pk[2], pk[3]};
    const unsigned short* vp0 = Vg + (32u * ks + 4u * G) * 128u + c; const unsigned short* vp1 = vp0 + 16 * 128;
#pragma unroll
    for (int u = 0; u < 8; ++u) {
      bf16x8 vf;
#pragma unroll
      for (int jj = 0; jj < 8; ++jj) vf[jj] = (short)(jj < 4 ? vp0 : vp1)[(jj & 3) * 128 + 16 * u];
      O[u] = mfma(vf, __builtin_bit_cast(bf16x8, pu), O[u]);
    }
  }
  ls += __shfl_xor(ls, 16); ls += __shfl_xor(ls, 32);
  const float inv = 1.0f / ls;
#pragma unroll
  for (int s = 0; s < 4; ++s) {
    u32x4 f = {cvtpk(O[2 * s][0] * inv, O[2 * s][1] * inv), cvtpk(O[2 * s][2] * inv, O[2 * s][3] * inv),
               cvtpk(O[2 * s + 1][0] * inv, O[2 * s + 1][1] * inv), cvtpk(O[2 * s + 1][2] * inv, O[2 * s + 1][3] * inv)};
    __builtin_amdgcn_raw_buffer_store_b128(f, dsrd, dst + s * 1024u + l * 16u, 0, 16);
  }
}

__global__ __launch_bounds__(512, 2) void k1_attn(const unsigned short* __restrict__ x, const unsigned short* __restrict__ kc,
                                                  const unsigned short* __restrict__ vc, const unsigned short* __restrict__ wq,
                                                  const unsigned short* __restrict__ wo, unsigned short* __restrict__ out, char* ws) {
  extern __shared__ __attribute__((aligned(16))) char lds[];
  TS(0);
  const unsigned lb = (unsigned)(size_t)lds;
  const unsigned tid = threadIdx.x, l = tid & 63u, c = l & 15u, G = l >> 4, q4 = (l >> 2) & 3u, p = l & 3u;
  const unsigned w = rfl(tid >> 6);
  const unsigned blk = blockIdx.x, g = blk & 7u, r_ = blk >> 3, j = r_ & 3u, b = r_ >> 2, h = 4u * g + j;
  const unsigned rot = ROT ? (b + 2u * j) % 9u : 0u;

  asm volatile("" : "+s"(x), "+s"(kc), "+s"(vc), "+s"(wq), "+s"(wo), "+s"(ws));
  const __amdgpu_buffer_rsrc_t srdF = __builtin_amdgcn_make_buffer_rsrc((void*)(ws + WS_FLAG), (short)0, 256 * 256, 0x00020000);
  const __amdgpu_buffer_rsrc_t srdOX = __builtin_amdgcn_make_buffer_rsrc((void*)(ws + WS_OX + (size_t)(b * NH + 4u * g) * 36864u), (short)0, 4 * 36864, 0x00020000);
  const __amdgpu_buffer_rsrc_t srdPX = __builtin_amdgcn_make_buffer_rsrc((void*)(ws + WS_PX + (size_t)(b * 4u + j) * 8u * PXB), (short)0, 8 * PXB, 0x00020000);
  unsigned finit, fadr;
  const __amdgpu_buffer_rsrc_t srdX = __builtin_amdgcn_make_buffer_rsrc((void*)(x + (size_t)b * SQ * DM), (short)0, SQ * DM * 2, 0x00020000);
  const __amdgpu_buffer_rsrc_t srdW = __builtin_amdgcn_make_buffer_rsrc((void*)(wq + (size_t)h * DH), (short)0, (DM * 4096 - h * DH) * 2, 0x00020000);
  const __amdgpu_buffer_rsrc_t srdK = __builtin_amdgcn_make_buffer_rsrc((void*)(kc + (size_t)(b * NH + h) * SK * DH), (short)0, SK * DH * 2, 0x00020000);
  const __amdgpu_buffer_rsrc_t srdV = __builtin_amdgcn_make_buffer_rsrc((void*)(vc + (size_t)(b * NH + h) * SK * DH), (short)0, SK * DH * 2, 0x00020000);

  const unsigned vx0 = (16u * w + (l >> 3)) * 1152u + (((l & 7u) ^ (G & 7u)) << 4);
  const unsigned vx1 = (16u * w + 8u + (l >> 3)) * 1152u + (((l & 7u) ^ ((4u + G) & 7u)) << 4);
  const unsigned vx9 = (128u + 2u * w + (l >> 5)) * 1152u + (((((l & 31u) >> 2) ^ w) & 7u) << 4) + ((l & 3u) << 2);
  const unsigned vw = (8u * w + G) * 8192u + ((((c >> 1) ^ (G | ((w & 1u) << 2)))) << 5) + ((c & 1u) << 4);
  const unsigned vkl = w * 2048u + l * 16u;
  const unsigned kw0 = lb + (8u * w + G) * 256u + ((c ^ (8u * (w & 1u) + G)) << 4);
  const unsigned kw1 = lb + (8u * w + 4u + G) * 256u + ((c ^ (8u * (w & 1u) + 4u + G)) << 4);
  const unsigned vv0 = (8u * w + G) * 256u + (((c >> 1) ^ G) << 5) + ((c & 1u) << 4);
  const unsigned vv1 = (8u * w + 4u + G) * 256u + (((c >> 1) ^ (4u + G)) << 5) + ((c & 1u) << 4);
  constexpr int KS[8] = {2, 3, 6, 2, 3, 6, 2, 3}, VS[8] = {7, 0, 1, 4, 5, 6, 2, 3};

  auto issueXW = [&](int i) { const unsigned s = lb + ((2 * i) & 7) * SLOT; unsigned kt = i + rot; kt = kt >= 9u ? kt - 9u : kt;
    dma16(s + w * 2048u, vx0, srdX, kt * 128u); dma16(s + w * 2048u + 1024u, vx1, srdX, kt * 128u); dma4(s + 16384u + w * 256u, vx9, srdX, kt * 128u);
    dma16(s + SLOT + w * 2048u, vw, srdW, kt * (64u * 8192u)); dma16(s + SLOT + w * 2048u + 1024u, vw, srdW, kt * (64u * 8192u) + 4u * 8192u); };
  u32x4 kp[8][2];
  auto loadK = [&](int t) {
    if (KV_NT) asm volatile("buffer_load_dwordx4 %0, %2, %3, %4 offen nt\n\tbuffer_load_dwordx4 %1, %2, %3, %4 offen offset:1024 nt" : "=&v"(kp[t][0]), "=&v"(kp[t][1]) : "v"(vkl), "s"(srdK), "s"(t * 16384) : "memory");
    else asm volatile("buffer_load_dwordx4 %0, %2, %3, %4 offen\n\tbuffer_load_dwordx4 %1, %2, %3, %4 offen offset:1024" : "=&v"(kp[t][0]), "=&v"(kp[t][1]) : "v"(vkl), "s"(srdK), "s"(t * 16384) : "memory");
  };
  auto writeK = [&](int t) {
    *(LAS u32x4*)(size_t)(kw0 + KS[t] * SLOT) = kp[t][0]; *(LAS u32x4*)(size_t)(kw1 + KS[t] * SLOT) = kp[t][1]; };
#define KWAIT(N, t) asm volatile("s_waitcnt vmcnt(%2)" : "+v"(kp[t][0]), "+v"(kp[t][1]) : "i"(N) : "memory")
  auto issueV = [&](int t) { const unsigned s = lb + VS[t] * SLOT + w * 2048u;
    dmakv(s, vv0, srdV, t * 16384); dmakv(s + 1024u, vv1, srdV, t * 16384); };

  constexpr int WS[8] = {0, 1, 7, 4, 5, 6, 7, 4};
  const __amdgpu_buffer_rsrc_t srdO = __builtin_amdgcn_make_buffer_rsrc((void*)(wo + (size_t)g * 512 * DM + j * 144), (short)0, (4096 * DM - g * 512 * DM - j * 144) * 2, 0x00020000);
  auto issueWO = [&](int i) {
    const unsigned ch0 = (2u * w) * 64u + l, ch1 = ch0 + 64u, dw = 4096u + w * 64u + l;
    const unsigned so = (((j + (i >> 1)) & 3u) * 2u + (i & 1)) * (64u * 1152u), s = lb + WS[i] * SLOT;
    dma16(s + w * 2048u, (ch0 / 18u) * 1152u + (ch0 % 18u) * 16u, srdO, so); dma16(s + w * 2048u + 1024u, (ch1 / 18u) * 1152u + (ch1 % 18u) * 16u, srdO, so);
    dma4(s + 16384u + w * 256u, (dw / 72u) * 1152u + (dw % 72u) * 4u, srdO, so); };
  auto issueWO6 = [&](int i) {
    const unsigned so = (((j + (i >> 1)) & 3u) * 2u + (i & 1)) * (64u * 1152u), s = lb + WS[i] * SLOT + (w - 1u) * 3072u;
#pragma unroll
    for (int e = 0; e < 3; ++e) { const unsigned ch = ((w - 1u) * 3u + e) * 64u + l; dma16(s + e * 1024u, (ch / 18u) * 1152u + (ch % 18u) * 16u, srdO, so); } };

  asm volatile("s_nop 4");
  issueXW(0); issueXW(1); issueXW(2); issueXW(3);

  const unsigned XCH = lb + 3u * SLOT + 16384u;
  const unsigned qg = w & 3u, hf = w >> 2, wv = 2u * qg + hf;
  const f32x4 zero4 = {0.f, 0.f, 0.f, 0.f};
  {
    f32x4 qa[2][4], qa9 = zero4;
#pragma unroll
    for (int t = 0; t < 4; ++t) { qa[0][t] = zero4; qa[1][t] = zero4; }
    const unsigned axo = lb + (32u * qg + c) * 128u + ((G ^ (c >> 1)) << 4);
    const unsigned ax9 = lb + (128u + c) * 128u + ((G ^ (c >> 1)) << 4);
    const unsigned awb = (8u * G + q4) * 256u + (((4u * hf + qg) ^ (q4 | ((G & 1u) << 2))) << 5) + 8u * p;
    bf16x8 af[2][4], bx[2][3];
    unsigned wb[2][4], xb[2][2], x9b[2][2];
#pragma unroll
    for (int hh = 0; hh < 2; ++hh) {
#pragma unroll
      for (int t = 0; t < 4; ++t) wb[hh][t] = opq(lb + (awb ^ (t << 5)) + hh * 4 * SLOT);
#pragma unroll
      for (int ks = 0; ks < 2; ++ks) { xb[hh][ks] = opq((axo ^ (ks << 6)) + hh * 4 * SLOT); x9b[hh][ks] = opq((ax9 ^ (ks << 6)) + hh * 4 * SLOT); }
    }
    auto rdA = [&](int gg, int t) { const int i = gg >> 1, ks = gg & 1, sl = (2 * i + 1) & 7;
      const unsigned a = wb[sl >> 2][t] + (sl & 3) * SLOT + ks * 8192;
      af[gg & 1][t] = cat(ldstr(a), ldstr(a + 1024)); };
    auto rdB = [&](int gg, int e) { const int i = gg >> 1, ks = gg & 1, sl = (2 * i) & 7;
      if (e < 2) bx[gg & 1][e] = lds128(xb[sl >> 2][ks] + (sl & 3) * SLOT + e * 2048); else bx[gg & 1][2] = lds128(x9b[sl >> 2][ks] + (sl & 3) * SLOT); };
    auto grp = [&](int gg) {
#pragma unroll
      for (int t = 0; t < 4; ++t) {
        qa[0][t] = mfma(af[gg & 1][t], bx[gg & 1][0], qa[0][t]);
        qa[1][t] = mfma(af[gg & 1][t], bx[gg & 1][1], qa[1][t]);
        if (t == 0) { qa9 = mfma(af[gg & 1][0], bx[gg & 1][2], qa9); PIN(qa9); }
        if (gg < 17) { if (t < 3) rdB(gg + 1, t); rdA(gg + 1, t); }
        SB();
      }
    };
    wait_bar<15>(); TS(1);
    rdB(0, 0); rdB(0, 1); rdB(0, 2);
#pragma unroll
    for (int t = 0; t < 4; ++t) rdA(0, t);
    SB();
#define LK(sg) do { _Pragma("unroll") for (int t_ = 0; t_ < 8; ++t_) if (SC.kseg[t_] == (sg)) loadK(t_); } while (0)
#define STAG 1
#define SEG(sg, ga, gb, XWI) do { if (STAG && hf == 0u) { if (XWI) issueXW(sg + 2); LK(sg); } \
      grp(ga); if (STAG == 2 && hf != 0u) { if (XWI) issueXW(sg + 2); LK(sg); } grp(gb); \
      if (STAG == 0 || (STAG == 1 && hf != 0u)) { if (XWI) issueXW(sg + 2); LK(sg); } } while (0)
    grp(0);  LK(0); wait_bar<cum(0) - posXW(1)>(); TS(2);
    SEG(1, 1, 2, 0);   wait_bar<cum(1) - posXW(2)>(); TS(3);
    SEG(2, 3, 4, 1);   wait_bar<cum(2) - posXW(3)>(); TS(4);
    SEG(3, 5, 6, 1);   wait_bar<cum(3) - posXW(4)>(); TS(5);
    SEG(4, 7, 8, 1);   wait_bar<cum(4) - posXW(5)>(); TS(6);
    SEG(5, 9, 10, 1);  wait_bar<cum(5) - posXW(6)>(); TS(7);
    SEG(6, 11, 12, 1); wait_bar<cum(6) - posXW(7)>(); TS(8);
    SEG(7, 13, 14, 0); wait_bar<cum(7) - posXW(8)>(); TS(9);
    SEG(8, 15, 16, 0); grp(17);
#undef SEG
#undef LK
    {
      const unsigned bj = (b * 4u + (l & 3u)) * 8u + g, bg = (b * 4u + j) * 8u + (l & 7u);
      unsigned a = blk * 256u;
      a = l < 40u ? blk * 256u + 128u + (l & 7u) * 4u : a;
      a = l < 32u ? blk * 256u : a;
      a = l < 28u ? blk * 256u + 64u + (l & 3u) * 4u : a;
      a = l < 24u ? bg * 256u + 128u + g * 4u : a;
      a = l < 16u ? bg * 256u + 32u : a;
      a = l < 8u ? bj * 256u + 64u + j * 4u : a;
      a = l < 4u ? bj * 256u : a;
      fadr = a;
    }
    asm volatile("buffer_load_dword %0, %1, %2, 0 offen sc1" : "=v"(finit) : "v"(fadr), "s"(srdF) : "memory");
    KWAIT(cl63(cum(8) + 1 - posK(0)), 0); writeK(0); KWAIT(cl63(cum(8) + 1 - posK(1)), 1); writeK(1);
    const unsigned QB = lb + 4u * SLOT;
#pragma unroll
    for (int e = 0; e < 2; ++e)
#pragma unroll
      for (int t = 0; t < 4; ++t) {
        const unsigned dt = 4u * hf + (t ^ qg);
        u32x2 v = {cvtpk(qa[e][t][0] * SL2E, qa[e][t][1] * SL2E), cvtpk(qa[e][t][2] * SL2E, qa[e][t][3] * SL2E)};
        *(LAS u32x2*)(size_t)(QB + (32u * qg + 16u * e + c) * 256u + (((2u * dt + (G >> 1)) ^ c) << 4) + (G & 1u) * 8u) = v;
      }
    { const unsigned dt = 4u * hf + qg;
      u32x2 v = {cvtpk(qa9[0] * SL2E, qa9[1] * SL2E), cvtpk(qa9[2] * SL2E, qa9[3] * SL2E)};
      *(LAS u32x2*)(size_t)(lb + MISC + c * 256u + (((2u * dt + (G >> 1)) ^ c) << 4) + (G & 1u) * 8u) = v; }
  }

  u32x4 P[8][2];
  float fsc[2], inv9;
  f32x4 mst;
  unsigned slow;
  {
    f32x4 lsum[2] = {zero4, zero4};
    bf16x8 qf[2][4], q9f[4], kf[3][4];
    f32x4 S[2][2][2];
    f32x4 ncv[2] = {zero4, zero4}, nc9 = zero4, S9f = zero4;
    float l9 = 0.f;
    const u32x4 onesu = {0x3F803F80u, 0x3F803F80u, 0x3F803F80u, 0x3F803F80u};
    const bf16x8 ones = __builtin_bit_cast(bf16x8, onesu);
    const unsigned tpar = (qg >> 1) ^ hf;
    const unsigned dutymask = (0x1111u << (2u * tpar + (qg & 1u)));
    const unsigned p9t = opq(lb + MISC + hf * 1024u + l * 16u + (qg & 1u) * 8u + tpar * 2048u);
    const unsigned akb = lb + c * 256u + ((G ^ c) << 4) + hf * 8192u;
    unsigned kb[2][4];
#pragma unroll
    for (int hh = 0; hh < 2; ++hh)
#pragma unroll
      for (int s = 0; s < 4; ++s) kb[hh][s] = opq((akb ^ (s << 6)) + hh * 4 * SLOT);
    auto rdK = [&](int un) { const int sl = KS[un >> 1];
#pragma unroll
      for (int s = 0; s < 4; ++s) kf[un % 3][s] = lds128(kb[sl >> 2][s] + (sl & 3) * SLOT + (un & 1) * 4096); };
    auto rowmax = [&](float m) {
      { auto r = __builtin_amdgcn_permlane32_swap(__float_as_uint(m), __float_as_uint(m), false, false); m = fmaxf(__uint_as_float(r[0]), __uint_as_float(r[1])); }
      { auto r = __builtin_amdgcn_permlane16_swap(__float_as_uint(m), __float_as_uint(m), false, false); m = fmaxf(__uint_as_float(r[0]), __uint_as_float(r[1])); }
      return m; };
    auto expack = [&](int T, int e) {
      const f32x4 a = S[T & 1][0][e], bq = S[T & 1][1][e];
      float e0 = __builtin_amdgcn_exp2f(a[0]), e1 = __builtin_amdgcn_exp2f(a[1]), e2 = __builtin_amdgcn_exp2f(a[2]), e3 = __builtin_amdgcn_exp2f(a[3]);
      float e4 = __builtin_amdgcn_exp2f(bq[0]), e5 = __builtin_amdgcn_exp2f(bq[1]), e6 = __builtin_amdgcn_exp2f(bq[2]), e7 = __builtin_amdgcn_exp2f(bq[3]);
      u32x4 f = {cvtpk(e0, e1), cvtpk(e2, e3), cvtpk(e4, e5), cvtpk(e6, e7)};
      P[T][e] = f;
      lsum[e] = mfma(ones, __builtin_bit_cast(bf16x8, f), lsum[e]);
      PIN(P[T][e]); PIN(lsum[e]);
    };
    auto exp9 = [&](f32x4 s, unsigned dst) {
      const float e0 = __builtin_amdgcn_exp2f(s[0]), e1 = __builtin_amdgcn_exp2f(s[1]), e2 = __builtin_amdgcn_exp2f(s[2]), e3 = __builtin_amdgcn_exp2f(s[3]);
      l9 += (e0 + e1) + (e2 + e3);
      u32x2 v = {cvtpk(e0, e1), cvtpk(e2, e3)};
      *(LAS u32x2*)(size_t)dst = v; };
    auto setref = [&]() {
#pragma unroll
      for (int e = 0; e < 2; ++e) {
        f32x4 (&Sp)[2][2] = S[0];
        float d = fmaxf(fmaxf(fmaxf(Sp[0][e][0], Sp[0][e][1]), fmaxf(Sp[0][e][2], Sp[0][e][3])), fmaxf(fmaxf(Sp[1][e][0], Sp[1][e][1]), fmaxf(Sp[1][e][2], Sp[1][e][3])));
        d = rowmax(d);
        Sp[0][e] -= d; Sp[1][e] -= d; ncv[e] -= d;
      }
      SB();
    };
    auto kunit = [&](int un) {
      const int t = un >> 1, i = un & 1;
      if (un + 2 < 16) rdK(un + 2);
      SB();
      f32x4 a0 = ncv[0], a1 = ncv[1];
#pragma unroll
      for (int s = 0; s < 4; ++s) { a0 = mfma(kf[un % 3][s], qf[0][s], a0); a1 = mfma(kf[un % 3][s], qf[1][s], a1); }
      S[t & 1][i][0] = a0; S[t & 1][i][1] = a1;
      if (t > 0) expack(t - 1, i);
      if (un == 8 || un == 12) exp9(S9f, p9t + (un / 2 - 2) * 2048u);
      SB();
      if (dutymask & (1u << un)) {
        f32x4 acc9 = nc9;
#pragma unroll
        for (int s = 0; s < 4; ++s) acc9 = mfma(kf[un % 3][s], q9f[s], acc9);
        S9f = acc9;
      }
      SB();
      if (un == 3) {
        const float m = rowmax(fmaxf(fmaxf(S9f[0], S9f[1]), fmaxf(S9f[2], S9f[3])));
        if (G == 0) *(LAS float*)(size_t)(XCH + w * 64u + c * 4u) = m;
        SB();
      }
      if (un == 1) setref();
    };
    lds_bar(); TS(10);
    {
      const unsigned qb = lb + 4u * SLOT + c * 256u;
#pragma unroll
      for (int s = 0; s < 4; ++s) {
        const unsigned o = (((4u * s + G) ^ c) << 4);
        qf[0][s] = lds128(qb + (2u * qg + hf) * 4096u + o);
        qf[1][s] = lds128(qb + (2u * qg + (1u - hf)) * 4096u + o);
        q9f[s] = lds128(lb + MISC + c * 256u + o);
      }
    }
    rdK(0); rdK(1);
    KWAIT(cl63(cum(8) + 1 - posK(2)), 2); writeK(2); SB();
    kunit(0); kunit(1); issueV(0); issueV(1); issueV(2);
    lds_bar(); TS(11); KWAIT(cl63(cum(8) + 7 - posK(3)), 3); writeK(3); kunit(2); kunit(3); issueV(3); issueV(4);
    lds_bar(); TS(12); KWAIT(cl63(cum(8) + 11 - posK(4)), 4); writeK(4);
    {
      float m = -3.0e38f;
#pragma unroll
      for (int i = 0; i < 8; ++i) m = fmaxf(m, *(const LAS float*)(size_t)(XCH + i * 64u + c * 4u));
      nc9 = (f32x4){-m, -m, -m, -m};
      exp9(S9f + nc9, p9t);
      SB();
    }
    kunit(4); kunit(5);
    lds_bar4(); TS(13); KWAIT(cl63(cum(8) + 11 - posK(5)), 5); writeK(5); kunit(6); kunit(7);
    lds_bar4(); TS(14); KWAIT(cl63(cum(8) + 11 - posK(6)), 6); writeK(6); kunit(8); kunit(9);
    lds_bar4(); TS(15); KWAIT(cl63(cum(8) + 11 - posK(7)), 7); writeK(7); kunit(10); kunit(11);
    lds_bar4(); TS(16); kunit(12); kunit(13); issueV(5);
    wait_bar<63>(); TS(17); kunit(14); kunit(15); issueV(6);
    expack(7, 0); expack(7, 1); exp9(S9f, p9t + 6 * 2048u);
    l9 += __shfl_xor(l9, 16); l9 += __shfl_xor(l9, 32);
    if (G == 0) *(LAS float*)(size_t)(XCH + 512u + w * 64u + c * 4u) = l9;
    {
      const bool bad = __any(!(lsum[0][0] <= THRP) || !(lsum[1][0] <= THRP) || !(l9 <= THRP));
      if (l == 0) *(LAS unsigned*)(size_t)(XCH + 1024u + w * 4u) = bad ? 1u : 0u; }
    if (G == 0) { const f32x4 st = {ncv[0][0], lsum[0][0], ncv[1][0], lsum[1][0]}; *(LAS f32x4*)(size_t)(lb + SLOT + 16384u + w * 256u + c * 16u) = st; }
    asm volatile("s_waitcnt vmcnt(12) lgkmcnt(0)\n\ts_barrier" : "+v"(finit) :: "memory"); TS(18);
    issueV(7);
    mst = (f32x4){ncv[0][0], lsum[0][0], ncv[1][0], lsum[1][0]};
    {
      const unsigned fl = *(const LAS unsigned*)(size_t)(XCH + 1024u + (l & 7u) * 4u);
      slow = rfl(__any(fl != 0u) ? 1u : 0u);
      if (__builtin_expect(slow != 0u, 0)) {
        const char* Kg = (const char*)(kc + (size_t)(b * NH + h) * SK * DH); const unsigned short* Vg = vc + (size_t)(b * NH + h) * SK * DH;
#pragma nounroll
        for (unsigned rep = 0; rep < (w == 0 ? 2u : 1u); ++rep)
          slow_tile_impl(rep ? q9f[0] : qf[0][0], rep ? q9f[1] : qf[0][1], rep ? q9f[2] : qf[0][2], rep ? q9f[3] : qf[0][3], Kg, Vg, srdOX, j * 36864u + (rep ? 8u : wv) * 4096u, l);
        asm volatile("s_waitcnt vmcnt(0) lgkmcnt(0)\n\ts_barrier" ::: "memory");
#pragma nounroll
        for (unsigned rep = 0; rep < (w == 0 ? 2u : 1u); ++rep)
#pragma unroll
          for (int s = 0; s < 4; ++s) { const unsigned o = (rep ? 8u : wv) * 4096u + s * 1024u + l * 16u;
            *(LAS u32x4*)(size_t)(lb + 2u * SLOT + o) = __builtin_amdgcn_raw_buffer_load_b128(srdOX, j * 36864u + o, 0, 16); }
        issueWO(0); issueWO(1); issueWO(2); issueWO(3);
        asm volatile("s_waitcnt vmcnt(0)\n\ts_barrier" ::: "memory");
      }
    }
  }
  SB();

  if (slow == 0u) {
  f32x4 O[2][8], O9[2] = {zero4, zero4};
#pragma unroll
  for (int u = 0; u < 8; ++u) { O[0][u] = zero4; O[1][u] = zero4; }
  {
    constexpr int PD = 6;
    const unsigned avb = (32u * hf + 4u * G + q4) * 256u + ((wv ^ (4u * (G & 1u) + q4)) << 5) + 8u * p;
    bf16x8 vf[8], p9[2];
    unsigned vb[2][8];
#pragma unroll
    for (int hh = 0; hh < 2; ++hh)
#pragma unroll
      for (int u = 0; u < 8; ++u) vb[hh][u] = opq(lb + (avb ^ (u << 5)) + hh * 4 * SLOT);
    const unsigned p9b = opq(lb + MISC + hf * 1024u + l * 16u);
    auto rdV = [&](int f) { const int t = f >> 3, u = f & 7, sl = VS[t];
      const unsigned a = vb[sl >> 2][u] + (sl & 3) * SLOT;
      vf[f & 7] = cat(ldstr(a), ldstr(a + 4096));
      if (u == 0) p9[t & 1] = lds128(p9b + t * 2048u); };
    auto vfr = [&](int f) { const int t = f >> 3, u = f & 7;
      O[0][u] = mfma(vf[f & 7], __builtin_bit_cast(bf16x8, P[t][0]), O[0][u]);
      O[1][u] = mfma(vf[f & 7], __builtin_bit_cast(bf16x8, P[t][1]), O[1][u]);
      if (u < 2) { O9[u] = mfma(vf[f & 7], p9[t & 1], O9[u]); PIN(O9[u]); }
      if (f + PD < 64) rdV(f + PD);
      SB(); };
#pragma unroll
    for (int f = 0; f < PD; ++f) rdV(f);
    SB(); TS(19);
#define VT(t, N, ts) vfr(8 * t); vfr(8 * t + 1); wait_bar<N>(); TS(ts); vfr(8 * t + 2); vfr(8 * t + 3); vfr(8 * t + 4); vfr(8 * t + 5); vfr(8 * t + 6); vfr(8 * t + 7);
    VT(0, 12, 20)
    {
      const f32x4 st = *(const LAS f32x4*)(size_t)(lb + SLOT + 16384u + (w ^ 4u) * 256u + c * 16u);
#pragma unroll
      for (int e = 0; e < 2; ++e) {
        const float na = mst[2 * e], nb = st[2 * (1 - e)], nm = fminf(na, nb);
        const float fa = __builtin_amdgcn_exp2f(nm - na), fb = __builtin_amdgcn_exp2f(nm - nb);
        fsc[e] = fa * __builtin_amdgcn_rcpf(mst[2 * e + 1] * fa + st[2 * (1 - e) + 1] * fb);
      }
      float l9t = 0.f;
#pragma unroll
      for (int i = 0; i < 8; ++i) l9t += *(const LAS float*)(size_t)(XCH + 512u + i * 64u + c * 4u);
      inv9 = __builtin_amdgcn_rcpf(l9t);
      PIN(fsc[0]); PIN(fsc[1]); PIN(inv9);
    }
    VT(1, 10, 21) issueWO(2); VT(2, 11, 22) issueWO(0); VT(3, 12, 23) issueWO(1); VT(4, 13, 24) issueWO(3); VT(5, 14, 25) VT(6, 12, 26)
#undef VT
#pragma unroll
    for (int f = 56; f < 64; ++f) vfr(f);
  }
  TS(27);

  {
    const unsigned mine = lb + 5u * SLOT + w * 4608u;
#pragma unroll
    for (int u = 0; u < 8; ++u) { const f32x4 o = O[1][u] * fsc[1]; const u32x2 v = {cvtpk(o[0], o[1]), cvtpk(o[2], o[3])}; *(LAS u32x2*)(size_t)(mine + ((u ^ wv) << 9) + l * 8u) = v; }
    { const f32x4 o = O9[1] * inv9; const u32x2 v = {cvtpk(o[0], o[1]), cvtpk(o[2], o[3])}; *(LAS u32x2*)(size_t)(mine + 4096u + l * 8u) = v; }
  }
  asm volatile("s_waitcnt vmcnt(0) lgkmcnt(0)\n\ts_barrier" ::: "memory");
  {
    const unsigned oth = lb + 5u * SLOT + (w ^ 4u) * 4608u;
    auto up = [&](u32x2 v) { return (f32x4){__uint_as_float(v[0] << 16), __uint_as_float(v[0] & 0xffff0000u), __uint_as_float(v[1] << 16), __uint_as_float(v[1] & 0xffff0000u)}; };
#pragma unroll
    for (int u = 0; u < 8; ++u) O[0][u] = O[0][u] * fsc[0] + up(*(const LAS u32x2*)(size_t)(oth + ((u ^ wv) << 9) + l * 8u));
    O9[0] = O9[0] * inv9 + up(*(const LAS u32x2*)(size_t)(oth + 4096u + l * 8u));
  }
  const bool odd = (wv & 1u) != 0;
#pragma unroll
  for (int s = 0; s < 4; ++s) {
    unsigned a0 = cvtpk(O[0][2 * s][0], O[0][2 * s][1]), a1 = cvtpk(O[0][2 * s][2], O[0][2 * s][3]);
    unsigned b0 = cvtpk(O[0][2 * s + 1][0], O[0][2 * s + 1][1]), b1 = cvtpk(O[0][2 * s + 1][2], O[0][2 * s + 1][3]);
    const u32x4 f = odd ? (u32x4){b0, b1, a0, a1} : (u32x4){a0, a1, b0, b1};
    const unsigned o = wv * 4096u + ((s ^ (wv >> 1)) << 10) + l * 16u;
    __builtin_amdgcn_raw_buffer_store_b128(f, srdOX, j * 36864u + o, 0, 16);
    *(LAS u32x4*)(size_t)(lb + 2u * SLOT + o) = f;
  }
  { const u32x2 v = {cvtpk(O9[0][0], O9[0][1]), cvtpk(O9[0][2], O9[0][3])};
    const unsigned o = 8u * 4096u + ((wv >> 1) << 10) + l * 16u + (wv & 1u) * 8u;
    __builtin_amdgcn_raw_buffer_store_b64(v, srdOX, j * 36864u + o, 0, 16);
    *(LAS u32x2*)(size_t)(lb + 2u * SLOT + o) = v; }
  }
  TS(28);

  unsigned hb = 1u;
  auto poll = [&](int stage) {
    const unsigned long long self1 = (1ull << j) | (1ull << (4u + j)), self2 = (1ull << (8u + g)) | (1ull << (16u + g));
    for (unsigned it = 0; it < 400000u; ++it) {
      unsigned v;
      asm volatile("buffer_load_dword %0, %1, %2, 0 offen sc1\n\ts_waitcnt vmcnt(0)" : "=v"(v) : "v"(fadr), "s"(srdF) : "memory");
      const unsigned long long ch = __ballot(v != finit) | self1 | self2;
      const bool ack1 = (ch & 0xF0ull) == 0xF0ull, ack2 = (ch & 0xFF0000ull) == 0xFF0000ull;
      if (stage == 1 && (ch & 0xFull) == 0xFull) break;
      if (stage == 2 && (ch & 0xFF00ull) == 0xFF00ull) break;
      if (stage == 3 && ack1 && ack2) break;
      if ((it & 63u) == 63u) {
        ++hb;
        if ((l == j && !ack1) || (stage >= 2 && l == 8u + g && !ack2)) __builtin_amdgcn_raw_buffer_store_b32(finit + hb, srdF, fadr, 0, 16);
      }
      __builtin_amdgcn_s_sleep(1);
    }
  };
  asm volatile("s_waitcnt vmcnt(0) lgkmcnt(0)\n\ts_barrier" ::: "memory");
  TS(29);
  if (w == 0) { if (l == j) __builtin_amdgcn_raw_buffer_store_b32(finit + 1u, srdF, fadr, 0, 16); }
  else if (w <= 6u) { issueWO6(4); issueWO6(5); }
  {
    f32x4 acc[2][4], acc8 = zero4, acc9 = zero4, acc99 = zero4;
#pragma unroll
    for (int u = 0; u < 4; ++u) { acc[0][u] = zero4; acc[1][u] = zero4; }
    const unsigned aob = lb + (4u * G + q4) * 288u + 8u * p;
    unsigned ab[2][5];
#pragma unroll
    for (int hh = 0; hh < 2; ++hh) {
#pragma unroll
      for (int u = 0; u < 4; ++u) ab[hh][u] = opq(aob + (4u * hf + (u ^ qg)) * 32u + hh * 4u * SLOT);
      ab[hh][4] = opq(aob + 256u + hh * 4u * SLOT);
    }
    bf16x8 ar[8], bq[2][16];
    auto pk = [&](f32x4 a, f32x4 bb) { return (u32x4){cvtpk(a[0], a[1]), cvtpk(a[2], a[3]), cvtpk(bb[0], bb[1]), cvtpk(bb[2], bb[3])}; };
    auto rdI = [&](int it) {
      const int hd = it / 20, idx = it % 20, s = hd < 3 ? idx / 5 : idx % 4, u = hd < 3 ? idx % 5 : idx / 4, sl = WS[2 * hd + (s >> 1)];
      const unsigned a = ab[sl >> 2][u] + (sl & 3) * SLOT + (s & 1) * 32u * 288u;
      ar[it & 7] = cat(ldstr(a), ldstr(a + 16u * 288u)); };
    auto doI = [&](int it, int stop) {
      const int hd = it / 20, idx = it % 20, s = hd < 3 ? idx / 5 : idx % 4, u = hd < 3 ? idx % 5 : idx / 4;
      const bf16x8* B = bq[hd & 1];
      if (u < 4) {
        acc[0][u] = mfma(ar[it & 7], B[s], acc[0][u]); acc[1][u] = mfma(ar[it & 7], B[4 + s], acc[1][u]);
        if (u == 0) acc9 = mfma(ar[it & 7], B[8 + s], acc9);
        if (it + 6 < stop) rdI(it + 6);
        if (hd == 3 && s == 1 && u > 0) __builtin_amdgcn_raw_buffer_store_b128(pk(acc[0][u - 1], acc[1][u - 1]), srdPX, g * PXB + (qg * 9u + 4u * hf + ((u - 1) ^ qg)) * 1024u + l * 16u, 0, 16);
        if (hd == 3 && s == 2 && u == 1) __builtin_amdgcn_raw_buffer_store_b128(pk(acc9, acc9), srdPX, g * PXB + (36u + 4u * hf + qg) * 1024u + l * 16u, 0, 16);
        SB();
      } else {
        acc8 = mfma(ar[it & 7], B[12 + s], acc8);
        acc99 = mfma(ar[it & 7], B[8 + s], acc99);
        if (it + 6 < stop) rdI(it + 6);
        if (hd == 3 && s == 1) __builtin_amdgcn_raw_buffer_store_b128(pk(acc[0][3], acc[1][3]), srdPX, g * PXB + (qg * 9u + 4u * hf + (3u ^ qg)) * 1024u + l * 16u, 0, 16);
        SB();
      } };
    auto rdB = [&](int hd) {
      const unsigned ib = lb + (hd == 2 ? 0u : 2u * SLOT) + l * 16u;
#pragma unroll
      for (int i = 0; i < 8; ++i) bq[hd & 1][i] = lds128(ib + qg * 8192u + i * 1024u);
#pragma unroll
      for (int i = 0; i < 4; ++i) { bq[hd & 1][8 + i] = lds128(ib + 8u * 4096u + i * 1024u); bq[hd & 1][12 + i] = lds128(ib + wv * 4096u + i * 1024u); } };
#define D5(i, st) doI(i, st); doI(i + 1, st); doI(i + 2, st); doI(i + 3, st); doI(i + 4, st);
#define D20(i, st) D5(i, st) D5(i + 5, st) D5(i + 10, st) D5(i + 15, st)
    rdB(0);
#pragma unroll
    for (int it = 0; it < 6; ++it) rdI(it);
    SB();
    D20(0, 26)
    if (w == 0) {
      poll(1);
      if (l >= 24u && l < 28u) __builtin_amdgcn_raw_buffer_store_b32(finit + 1u, srdF, fadr, 0, 16);
    }
    asm volatile("s_waitcnt vmcnt(0)\n\ts_barrier" ::: "memory");
    TS(30);
    {
      u32x4 xo[3][4]; u32x2 x9[3];
#pragma unroll
      for (int hd = 1; hd < 4; ++hd) {
        const unsigned so = ((j + hd) & 3u) * 36864u;
#pragma unroll
        for (int i = 0; i < 4; ++i) xo[hd - 1][i] = __builtin_amdgcn_raw_buffer_load_b128(srdOX, w * 4096u + l * 16u + i * 1024u, so, 16);
        x9[hd - 1] = __builtin_amdgcn_raw_buffer_load_b64(srdOX, 8u * 4096u + w * 512u + l * 8u, so, 16);
      }
      auto wrI = [&](int hd) { const unsigned ib = lb + (hd == 2 ? 0u : 2u * SLOT);
#pragma unroll
        for (int i = 0; i < 4; ++i) *(LAS u32x4*)(size_t)(ib + w * 4096u + l * 16u + i * 1024u) = xo[hd - 1][i];
        *(LAS u32x2*)(size_t)(ib + 8u * 4096u + w * 512u + l * 8u) = x9[hd - 1]; };
      wrI(1); wrI(2);
      lds_bar();
      TS(36);
      rdB(1); rdB(2); SB();
      D20(20, 46)
      lds_bar();
      if (w >= 1u && w <= 6u) { issueWO6(6); issueWO6(7); }
      wrI(3); SB();
      D20(40, 60)
      asm volatile("s_waitcnt vmcnt(0) lgkmcnt(0)\n\ts_barrier" ::: "memory");
      rdB(3);
#pragma unroll
      for (int it = 60; it < 66; ++it) rdI(it);
      SB();
      D20(60, 80)
    }
#undef D20
#undef D5
    { const u32x2 v = {cvtpk(acc8[0], acc8[1]), cvtpk(acc8[2], acc8[3])};
      __builtin_amdgcn_raw_buffer_store_b64(v, srdPX, g * PXB + (qg * 9u + 8u) * 1024u + l * 16u + hf * 8u, 0, 16); }
    if (w == 0u) __builtin_amdgcn_raw_buffer_store_b128(pk(acc99, acc99), srdPX, g * PXB + 44u * 1024u + l * 16u, 0, 16);
  }
  TS(31);
  asm volatile("s_waitcnt vmcnt(0)\n\ts_barrier" ::: "memory");
  TS(32);
  if (w == 0) {
    if (l == 8u + g) __builtin_amdgcn_raw_buffer_store_b32(finit + 1u, srdF, fadr, 0, 16);
    poll(2);
    if (l >= 32u && l < 40u) __builtin_amdgcn_raw_buffer_store_b32(finit + 1u, srdF, fadr, 0, 16);
  }
  asm volatile("s_barrier" ::: "memory");
  TS(33);
  {
    const unsigned un = g + 8u * (w - 1u);
    if (w >= 1u && un < 45u) {
      u32x4 pv[8];
#pragma unroll
      for (int gg = 0; gg < 8; ++gg) pv[gg] = __builtin_amdgcn_raw_buffer_load_b128(srdPX, un * 1024u + l * 16u, gg * PXB, 16);
      float sm[8];
#pragma unroll
      for (int k = 0; k < 4; ++k) {
        float lo = 0.f, hi = 0.f;
#pragma unroll
        for (int gg = 0; gg < 8; ++gg) { lo += __uint_as_float(pv[gg][k] << 16); hi += __uint_as_float(pv[gg][k] & 0xffff0000u); }
        sm[2 * k] = lo; sm[2 * k + 1] = hi;
      }
      const unsigned qa = un < 36u ? 2u * (un / 9u) : 8u, nt = un < 36u ? un % 9u : un - 36u;
      const u32x2 va = {cvtpk(sm[0], sm[1]), cvtpk(sm[2], sm[3])}, vb = {cvtpk(sm[4], sm[5]), cvtpk(sm[6], sm[7])};
      unsigned short* op = out + (size_t)(b * SQ + qa * 16u + c) * DM + j * 144u + nt * 16u + G * 4u;
      *(u32x2*)op = va;
      if (un < 36u) *(u32x2*)(op + 16 * DM) = vb;
    }
  }
  TS(34);
  if (w == 0) poll(3);
}

extern "C" void kernel_launch(void* const* d_in, const int* in_sizes, int n_in, void* d_out, int out_size, void* d_ws, size_t ws_size, hipStream_t stream) {
  const unsigned short* x = (const unsigned short*)d_in[0];
  const unsigned short* kc = (const unsigned short*)d_in[1];
  const unsigned short* vc = (const unsigned short*)d_in[2];
  const unsigned short* wq = (const unsigned short*)d_in[3];
  const unsigned short* wo = (const unsigned short*)d_in[4];
  (void)hipFuncSetAttribute((const void*)k1_attn, hipFuncAttributeMaxDynamicSharedMemorySize, LDS_BYTES);
  k1_attn<<<256, 512, LDS_BYTES, stream>>>(x, kc, vc, wq, wo, (unsigned short*)d_out, (char*)d_ws);
}
```

```cpp
#include <hip/hip_runtime.h>
#include <hip/hip_bf16.h>

#define LAS __attribute__((address_space(3)))
#define DI __device__ __forceinline__
typedef short bf16x8 __attribute__((ext_vector_type(8)));
typedef short s16x4 __attribute__((ext_vector_type(4)));
typedef float f32x4 __attribute__((ext_vector_type(4)));
typedef float f32x2 __attribute__((ext_vector_type(2)));
typedef __bf16 bf16x2 __attribute__((ext_vector_type(2)));
typedef unsigned u32x4 __attribute__((ext_vector_type(4)));
typedef unsigned u32x2 __attribute__((ext_vector_type(2)));

constexpr int SQ = 144, DM = 576, NH = 32, SK = 512, DH = 128;
constexpr unsigned SLOT = 18432, MISC = 8 * SLOT, LDS_BYTES = MISC + 16384;
#define THRP 1.099511627776e12f
constexpr unsigned PXB = 45 * 1024;
constexpr size_t WS_OX = 0, WS_PX = (size_t)16 << 20, WS_FLAG = (size_t)64 << 20;
constexpr float SL2E = 0.08838834764831845f * 1.4426950408889634f;

DI unsigned rfl(unsigned x) { return __builtin_amdgcn_readfirstlane(x); }
DI unsigned cvtpk(float lo, float hi) { f32x2 v = {lo, hi}; bf16x2 b = __builtin_convertvector(v, bf16x2); return __builtin_bit_cast(unsigned, b); }
DI bf16x8 lds128(unsigned a) { return *(const LAS bf16x8*)(size_t)a; }
DI s16x4 ldstr(unsigned a) { return __builtin_amdgcn_ds_read_tr16_b64_v4i16((LAS s16x4*)(size_t)a); }
DI bf16x8 cat(s16x4 lo, s16x4 hi) { return __builtin_shufflevector(lo, hi, 0, 1, 2, 3, 4, 5, 6, 7); }
DI f32x4 mfma(bf16x8 a, bf16x8 b, f32x4 c) { return __builtin_amdgcn_mfma_f32_16x16x32_bf16(a, b, c, 0, 0, 0); }
#define SB() __builtin_amdgcn_sched_barrier(0)
DI unsigned opq(unsigned x) { asm volatile("" : "+v"(x)); return x; }
#define PIN(x) asm volatile("" : "+v"(x))

DI void dma16(unsigned ldsdst, unsigned voff, __amdgpu_buffer_rsrc_t srd, unsigned soff) {
  asm volatile("s_mov_b32 m0, %0\n\ts_nop 0\n\tbuffer_load_dwordx4 %1, %2, %3 offen lds" :: "s"(ldsdst), "v"(voff), "s"(srd), "s"(soff) : "memory");
}
DI void dma16nt(unsigned ldsdst, unsigned voff, __amdgpu_buffer_rsrc_t srd, unsigned soff) {
  asm volatile("s_mov_b32 m0, %0\n\ts_nop 0\n\tbuffer_load_dwordx4 %1, %2, %3 offen nt lds" :: "s"(ldsdst), "v"(voff), "s"(srd), "s"(soff) : "memory");
}
DI void dma4(unsigned ldsdst, unsigned voff, __amdgpu_buffer_rsrc_t srd, unsigned soff) {
  asm volatile("s_mov_b32 m0, %0\n\ts_nop 0\n\tbuffer_load_dword %1, %2, %3 offen lds" :: "s"(ldsdst), "v"(voff), "s"(srd), "s"(soff) : "memory");
}
template <int N> DI void wait_bar() { asm volatile("s_waitcnt vmcnt(%0)\n\ts_barrier" :: "i"(N) : "memory"); }
template <int N> DI void wait_bar_l() { asm volatile("s_waitcnt vmcnt(%0) lgkmcnt(0)\n\ts_barrier" :: "i"(N) : "memory"); }
DI void lds_bar4() { asm volatile("s_waitcnt lgkmcnt(4)\n\ts_barrier" ::: "memory"); }
DI void lds_bar() { asm volatile("s_waitcnt lgkmcnt(0)\n\ts_barrier" ::: "memory"); }

#define ROT 1
#define KV_NT 1
DI void dmakv(unsigned ldsdst, unsigned voff, __amdgpu_buffer_rsrc_t srd, unsigned soff) {
  if (KV_NT) dma16nt(ldsdst, voff, srd, soff); else dma16(ldsdst, voff, srd, soff);
}

#define KSEG {1, 3, 5, 6, 7, 7, 8, 8}
struct Sched { int kseg[8]; };
constexpr Sched SC = {KSEG};
constexpr int nK(int s) { int n = 0; for (int t = 0; t < 8; ++t) n += SC.kseg[t] == s; return n; }
constexpr int cum(int s) { int n = 20; for (int i = 0; i <= s; ++i) n += ((i >= 2 && i <= 6) ? 5 : 0) + 2 * nK(i); return n; }
constexpr int posXW(int n) { return n <= 3 ? 5 * (n + 1) : cum(n - 3) + 5; }
constexpr int posK(int t) { int s = SC.kseg[t], n = cum(s - 1) + ((s >= 2 && s <= 6) ? 5 : 0); for (int i = 0; i <= t; ++i) n += (SC.kseg[i] == s) ? 2 : 0; return n; }
constexpr int cl63(int n) { return n > 63 ? 63 : n; }

DI void slow_tile_impl(bf16x8 q0, bf16x8 q1, bf16x8 q2, bf16x8 q3, const char* Kg, const unsigned short* Vg, __amdgpu_buffer_rsrc_t dsrd, unsigned dst, unsigned l) {
  const unsigned c = l & 15u, G = l >> 4;
  const f32x4 zero4 = {0.f, 0.f, 0.f, 0.f};
  float m = -3.0e38f;
#pragma nounroll
  for (int kt = 0; kt < 32; ++kt) {
    const char* kp = Kg + (16u * kt + c) * 256u + G * 16u;
    f32x4 a = mfma(*(const bf16x8*)kp, q0, zero4); a = mfma(*(const bf16x8*)(kp + 64), q1, a); a = mfma(*(const bf16x8*)(kp + 128), q2, a); a = mfma(*(const bf16x8*)(kp + 192), q3, a);
    m = fmaxf(fmaxf(m, fmaxf(a[0], a[1])), fmaxf(a[2], a[3]));
  }
  m = fmaxf(m, __shfl_xor(m, 16)); m = fmaxf(m, __shfl_xor(m, 32));
  f32x4 O[8];
#pragma unroll
  for (int u = 0; u < 8; ++u) O[u] = zero4;
  float ls = 0.f;
#pragma nounroll
  for (int ks = 0; ks < 16; ++ks) {
    unsigned pk[4];
#pragma unroll
    for (int hb = 0; hb < 2; ++hb) {
      const char* kp = Kg + (32u * ks + 16u * hb + c) * 256u + G * 16u;
      f32x4 a = mfma(*(const bf16x8*)kp, q0, zero4); a = mfma(*(const bf16x8*)(kp + 64), q1, a); a = mfma(*(const bf16x8*)(kp + 128), q2, a); a = mfma(*(const bf16x8*)(kp + 192), q3, a);
      const float e0 = __builtin_amdgcn_exp2f(a[0] - m), e1 = __builtin_amdgcn_exp2f(a[1] - m), e2 = __builtin_amdgcn_exp2f(a[2] - m), e3 = __builtin_amdgcn_exp2f(a[3] - m);
      ls += (e0 + e1) + (e2 + e3);
      pk[2 * hb] = cvtpk(e0, e1); pk[2 * hb + 1] = cvtpk(e2, e3);
    }
    const u32x4 pu = {pk[0], pk[1], pk[2], pk[3]};
    const unsigned short* vp0 = Vg + (32u * ks + 4u * G) * 128u + c; const unsigned short* vp1 = vp0 + 16 * 128;
#pragma unroll
    for (int u = 0; u < 8; ++u) {
      bf16x8 vf;
#pragma unroll
      for (int jj = 0; jj < 8; ++jj) vf[jj] = (short)(jj < 4 ? vp0 : vp1)[(jj & 3) * 128 + 16 * u];
      O[u] = mfma(vf, __builtin_bit_cast(bf16x8, pu), O[u]);
    }
  }
  ls += __shfl_xor(ls, 16); ls += __shfl_xor(ls, 32);
  const float inv = 1.0f / ls;
#pragma unroll
  for (int s = 0; s < 4; ++s) {
    u32x4 f = {cvtpk(O[2 * s][0] * inv, O[2 * s][1] * inv), cvtpk(O[2 * s][2] * inv, O[2 * s][3] * inv),
               cvtpk(O[2 * s + 1][0] * inv, O[2 * s + 1][1] * inv), cvtpk(O[2 * s + 1][2] * inv, O[2 * s + 1][3] * inv)};
    __builtin_amdgcn_raw_buffer_store_b128(f, dsrd, dst + s * 1024u + l * 16u, 0, 16);
  }
}

__global__ __launch_bounds__(512, 2) void k1_attn(const unsigned short* __restrict__ x, const unsigned short* __restrict__ kc,
                                                  const unsigned short* __restrict__ vc, const unsigned short* __restrict__ wq,
                                                  const unsigned short* __restrict__ wo, unsigned short* __restrict__ out, char* ws) {
  extern __shared__ __attribute__((aligned(16))) char lds[];

  const unsigned lb = (unsigned)(size_t)lds;
  const unsigned tid = threadIdx.x, l = tid & 63u, c = l & 15u, G = l >> 4, q4 = (l >> 2) & 3u, p = l & 3u;
  const unsigned w = rfl(tid >> 6);
  const unsigned blk = blockIdx.x, g = blk & 7u, r_ = blk >> 3, j = r_ & 3u, b = r_ >> 2, h = 4u * g + j;
  const unsigned rot = ROT ? (b + 2u * j) % 9u : 0u;

  asm volatile("" : "+s"(x), "+s"(kc), "+s"(vc), "+s"(wq), "+s"(wo), "+s"(ws));
  const __amdgpu_buffer_rsrc_t srdF = __builtin_amdgcn_make_buffer_rsrc((void*)(ws + WS_FLAG), (short)0, 256 * 256, 0x00020000);
  const __amdgpu_buffer_rsrc_t srdOX = __builtin_amdgcn_make_buffer_rsrc((void*)(ws + WS_OX + (size_t)(b * NH + 4u * g) * 36864u), (short)0, 4 * 36864, 0x00020000);
  const __amdgpu_buffer_rsrc_t srdPX = __builtin_amdgcn_make_buffer_rsrc((void*)(ws + WS_PX + (size_t)(b * 4u + j) * 8u * PXB), (short)0, 8 * PXB, 0x00020000);
  unsigned finit, fadr;
  const __amdgpu_buffer_rsrc_t srdX = __builtin_amdgcn_make_buffer_rsrc((void*)(x + (size_t)b * SQ * DM), (short)0, SQ * DM * 2, 0x00020000);
  const __amdgpu_buffer_rsrc_t srdW = __builtin_amdgcn_make_buffer_rsrc((void*)(wq + (size_t)h * DH), (short)0, (DM * 4096 - h * DH) * 2, 0x00020000);
  const __amdgpu_buffer_rsrc_t srdK = __builtin_amdgcn_make_buffer_rsrc((void*)(kc + (size_t)(b * NH + h) * SK * DH), (short)0, SK * DH * 2, 0x00020000);
  const __amdgpu_buffer_rsrc_t srdV = __builtin_amdgcn_make_buffer_rsrc((void*)(vc + (size_t)(b * NH + h) * SK * DH), (short)0, SK * DH * 2, 0x00020000);

  const unsigned vx0 = (16u * w + (l >> 3)) * 1152u + (((l & 7u) ^ (G & 7u)) << 4);
  const unsigned vx1 = (16u * w + 8u + (l >> 3)) * 1152u + (((l & 7u) ^ ((4u + G) & 7u)) << 4);
  const unsigned vx9 = (128u + 2u * w + (l >> 5)) * 1152u + (((((l & 31u) >> 2) ^ w) & 7u) << 4) + ((l & 3u) << 2);
  const unsigned vw = (8u * w + G) * 8192u + ((((c >> 1) ^ (G | ((w & 1u) << 2)))) << 5) + ((c & 1u) << 4);
  const unsigned vkl = w * 2048u + l * 16u;
  const unsigned kw0 = lb + (8u * w + G) * 256u + ((c ^ (8u * (w & 1u) + G)) << 4);
  const unsigned kw1 = lb + (8u * w + 4u + G) * 256u + ((c ^ (8u * (w & 1u) + 4u + G)) << 4);
  const unsigned vv0 = (8u * w + G) * 256u + (((c >> 1) ^ G) << 5) + ((c & 1u) << 4);
  const unsigned vv1 = (8u * w + 4u + G) * 256u + (((c >> 1) ^ (4u + G)) << 5) + ((c & 1u) << 4);
  constexpr int KS[8] = {2, 3, 6, 2, 3, 6, 2, 3}, VS[8] = {7, 0, 1, 4, 5, 6, 2, 3};

  auto issueXW = [&](int i) { const unsigned s = lb + ((2 * i) & 7) * SLOT; unsigned kt = i + rot; kt = kt >= 9u ? kt - 9u : kt;
    dma16(s + w * 2048u, vx0, srdX, kt * 128u); dma16(s + w * 2048u + 1024u, vx1, srdX, kt * 128u); dma4(s + 16384u + w * 256u, vx9, srdX, kt * 128u);
    dma16(s + SLOT + w * 2048u, vw, srdW, kt * (64u * 8192u)); dma16(s + SLOT + w * 2048u + 1024u, vw, srdW, kt * (64u * 8192u) + 4u * 8192u); };
  u32x4 kp[8][2];
  auto loadK = [&](int t) {
    if (KV_NT) asm volatile("buffer_load_dwordx4 %0, %2, %3, %4 offen nt\n\tbuffer_load_dwordx4 %1, %2, %3, %4 offen offset:1024 nt" : "=&v"(kp[t][0]), "=&v"(kp[t][1]) : "v"(vkl), "s"(srdK), "s"(t * 16384) : "memory");
    else asm volatile("buffer_load_dwordx4 %0, %2, %3, %4 offen\n\tbuffer_load_dwordx4 %1, %2, %3, %4 offen offset:1024" : "=&v"(kp[t][0]), "=&v"(kp[t][1]) : "v"(vkl), "s"(srdK), "s"(t * 16384) : "memory");
  };
  auto writeK = [&](int t) {
    *(LAS u32x4*)(size_t)(kw0 + KS[t] * SLOT) = kp[t][0]; *(LAS u32x4*)(size_t)(kw1 + KS[t] * SLOT) = kp[t][1]; };
#define KWAIT(N, t) asm volatile("s_waitcnt vmcnt(%2)" : "+v"(kp[t][0]), "+v"(kp[t][1]) : "i"(N) : "memory")
  auto issueV = [&](int t) { const unsigned s = lb + VS[t] * SLOT + w * 2048u;
    dmakv(s, vv0, srdV, t * 16384); dmakv(s + 1024u, vv1, srdV, t * 16384); };

  constexpr int WS[8] = {0, 1, 7, 4, 5, 6, 7, 4};
  const __amdgpu_buffer_rsrc_t srdO = __builtin_amdgcn_make_buffer_rsrc((void*)(wo + (size_t)g * 512 * DM + j * 144), (short)0, (4096 * DM - g * 512 * DM - j * 144) * 2, 0x00020000);
  auto issueWO = [&](int i) {
    const unsigned ch0 = (2u * w) * 64u + l, ch1 = ch0 + 64u, dw = 4096u + w * 64u + l;
    const unsigned so = (((j + (i >> 1)) & 3u) * 2u + (i & 1)) * (64u * 1152u), s = lb + WS[i] * SLOT;
    dma16(s + w * 2048u, (ch0 / 18u) * 1152u + (ch0 % 18u) * 16u, srdO, so); dma16(s + w * 2048u + 1024u, (ch1 / 18u) * 1152u + (ch1 % 18u) * 16u, srdO, so);
    dma4(s + 16384u + w * 256u, (dw / 72u) * 1152u + (dw % 72u) * 4u, srdO, so); };
  auto issueWO6 = [&](int i) {
    const unsigned so = (((j + (i >> 1)) & 3u) * 2u + (i & 1)) * (64u * 1152u), s = lb + WS[i] * SLOT + (w - 1u) * 3072u;
#pragma unroll
    for (int e = 0; e < 3; ++e) { const unsigned ch = ((w - 1u) * 3u + e) * 64u + l; dma16(s + e * 1024u, (ch / 18u) * 1152u + (ch % 18u) * 16u, srdO, so); } };

  asm volatile("s_nop 4");
  issueXW(0); issueXW(1); issueXW(2); issueXW(3);

  const unsigned XCH = lb + 3u * SLOT + 16384u;
  const unsigned qg = w & 3u, hf = w >> 2, wv = 2u * qg + hf;
  const f32x4 zero4 = {0.f, 0.f, 0.f, 0.f};
  {
    f32x4 qa[2][4], qa9 = zero4;
#pragma unroll
    for (int t = 0; t < 4; ++t) { qa[0][t] = zero4; qa[1][t] = zero4; }
    const unsigned axo = lb + (32u * qg + c) * 128u + ((G ^ (c >> 1)) << 4);
    const unsigned ax9 = lb + (128u + c) * 128u + ((G ^ (c >> 1)) << 4);
    const unsigned awb = (8u * G + q4) * 256u + (((4u * hf + qg) ^ (q4 | ((G & 1u) << 2))) << 5) + 8u * p;
    bf16x8 af[2][4], bx[2][3];
    unsigned wb[2][4], xb[2][2], x9b[2][2];
#pragma unroll
    for (int hh = 0; hh < 2; ++hh) {
#pragma unroll
      for (int t = 0; t < 4; ++t) wb[hh][t] = opq(lb + (awb ^ (t << 5)) + hh * 4 * SLOT);
#pragma unroll
      for (int ks = 0; ks < 2; ++ks) { xb[hh][ks] = opq((axo ^ (ks << 6)) + hh * 4 * SLOT); x9b[hh][ks] = opq((ax9 ^ (ks << 6)) + hh * 4 * SLOT); }
    }
    auto rdA = [&](int gg, int t) { const int i = gg >> 1, ks = gg & 1, sl = (2 * i + 1) & 7;
      const unsigned a = wb[sl >> 2][t] + (sl & 3) * SLOT + ks * 8192;
      af[gg & 1][t] = cat(ldstr(a), ldstr(a + 1024)); };
    auto rdB = [&](int gg, int e) { const int i = gg >> 1, ks = gg & 1, sl = (2 * i) & 7;
      if (e < 2) bx[gg & 1][e] = lds128(xb[sl >> 2][ks] + (sl & 3) * SLOT + e * 2048); else bx[gg & 1][2] = lds128(x9b[sl >> 2][ks] + (sl & 3) * SLOT); };
    auto grp = [&](int gg) {
#pragma unroll
      for (int t = 0; t < 4; ++t) {
        qa[0][t] = mfma(af[gg & 1][t], bx[gg & 1][0], qa[0][t]);
        qa[1][t] = mfma(af[gg & 1][t], bx[gg & 1][1], qa[1][t]);
        if (t == 0) { qa9 = mfma(af[gg & 1][0], bx[gg & 1][2], qa9); PIN(qa9); }
        if (gg < 17) { if (t < 3) rdB(gg + 1, t); rdA(gg + 1, t); }
        SB();
      }
    };
    wait_bar<15>();
    rdB(0, 0); rdB(0, 1); rdB(0, 2);
#pragma unroll
    for (int t = 0; t < 4; ++t) rdA(0, t);
    SB();
#define LK(sg) do { _Pragma("unroll") for (int t_ = 0; t_ < 8; ++t_) if (SC.kseg[t_] == (sg)) loadK(t_); } while (0)
#define STAG 1
#define SEG(sg, ga, gb, XWI) do { if (STAG && hf == 0u) { if (XWI) issueXW(sg + 2); LK(sg); } \
      grp(ga); if (STAG == 2 && hf != 0u) { if (XWI) issueXW(sg + 2); LK(sg); } grp(gb); \
      if (STAG == 0 || (STAG == 1 && hf != 0u)) { if (XWI) issueXW(sg + 2); LK(sg); } } while (0)
    grp(0);  LK(0); wait_bar<cum(0) - posXW(1)>();
    SEG(1, 1, 2, 0);   wait_bar<cum(1) - posXW(2)>();
    SEG(2, 3, 4, 1);   wait_bar<cum(2) - posXW(3)>();
    SEG(3, 5, 6, 1);   wait_bar<cum(3) - posXW(4)>();
    SEG(4, 7, 8, 1);   wait_bar<cum(4) - posXW(5)>();
    SEG(5, 9, 10, 1);  wait_bar<cum(5) - posXW(6)>();
    SEG(6, 11, 12, 1); wait_bar<cum(6) - posXW(7)>();
    SEG(7, 13, 14, 0); wait_bar<cum(7) - posXW(8)>();
    SEG(8, 15, 16, 0); grp(17);
#undef SEG
#undef LK
    {
      const unsigned bj = (b * 4u + (l & 3u)) * 8u + g, bg = (b * 4u + j) * 8u + (l & 7u);
      unsigned a = blk * 256u;
      a = l < 40u ? blk * 256u + 128u + (l & 7u) * 4u : a;
      a = l < 32u ? blk * 256u : a;
      a = l < 28u ? blk * 256u + 64u + (l & 3u) * 4u : a;
      a = l < 24u ? bg * 256u + 128u + g * 4u : a;
      a = l < 16u ? bg * 256u + 32u : a;
      a = l < 8u ? bj * 256u + 64u + j * 4u : a;
      a = l < 4u ? bj * 256u : a;
      fadr = a;
    }
    asm volatile("buffer_load_dword %0, %1, %2, 0 offen sc1" : "=v"(finit) : "v"(fadr), "s"(srdF) : "memory");
    KWAIT(cl63(cum(8) + 1 - posK(0)), 0); writeK(0); KWAIT(cl63(cum(8) + 1 - posK(1)), 1); writeK(1);
    const unsigned QB = lb + 4u * SLOT;
#pragma unroll
    for (int e = 0; e < 2; ++e)
#pragma unroll
      for (int t = 0; t < 4; ++t) {
        const unsigned dt = 4u * hf + (t ^ qg);
        u32x2 v = {cvtpk(qa[e][t][0] * SL2E, qa[e][t][1] * SL2E), cvtpk(qa[e][t][2] * SL2E, qa[e][t][3] * SL2E)};
        *(LAS u32x2*)(size_t)(QB + (32u * qg + 16u * e + c) * 256u + (((2u * dt + (G >> 1)) ^ c) << 4) + (G & 1u) * 8u) = v;
      }
    { const unsigned dt = 4u * hf + qg;
      u32x2 v = {cvtpk(qa9[0] * SL2E, qa9[1] * SL2E), cvtpk(qa9[2] * SL2E, qa9[3] * SL2E)};
      *(LAS u32x2*)(size_t)(lb + MISC + c * 256u + (((2u * dt + (G >> 1)) ^ c) << 4) + (G & 1u) * 8u) = v; }
  }

  u32x4 P[8][2];
  float fsc[2], inv9;
  f32x4 mst;
  unsigned slow;
  {
    f32x4 lsum[2] = {zero4, zero4};
    bf16x8 qf[2][4], q9f[4], kf[3][4];
    f32x4 S[2][2][2];
    f32x4 ncv[2] = {zero4, zero4}, nc9 = zero4, S9f = zero4;
    float l9 = 0.f;
    const u32x4 onesu = {0x3F803F80u, 0x3F803F80u, 0x3F803F80u, 0x3F803F80u};
    const bf16x8 ones = __builtin_bit_cast(bf16x8, onesu);
    const unsigned tpar = (qg >> 1) ^ hf;
    const unsigned dutymask = (0x1111u << (2u * tpar + (qg & 1u)));
    const unsigned p9t = opq(lb + MISC + hf * 1024u + l * 16u + (qg & 1u) * 8u + tpar * 2048u);
    const unsigned akb = lb + c * 256u + ((G ^ c) << 4) + hf * 8192u;
    unsigned kb[2][4];
#pragma unroll
    for (int hh = 0; hh < 2; ++hh)
#pragma unroll
      for (int s = 0; s < 4; ++s) kb[hh][s] = opq((akb ^ (s << 6)) + hh * 4 * SLOT);
    auto rdK = [&](int un) { const int sl = KS[un >> 1];
#pragma unroll
      for (int s = 0; s < 4; ++s) kf[un % 3][s] = lds128(kb[sl >> 2][s] + (sl & 3) * SLOT + (un & 1) * 4096); };
    auto rowmax = [&](float m) {
      { auto r = __builtin_amdgcn_permlane32_swap(__float_as_uint(m), __float_as_uint(m), false, false); m = fmaxf(__uint_as_float(r[0]), __uint_as_float(r[1])); }
      { auto r = __builtin_amdgcn_permlane16_swap(__float_as_uint(m), __float_as_uint(m), false, false); m = fmaxf(__uint_as_float(r[0]), __uint_as_float(r[1])); }
      return m; };
    auto expack = [&](int T, int e) {
      const f32x4 a = S[T & 1][0][e], bq = S[T & 1][1][e];
      float e0 = __builtin_amdgcn_exp2f(a[0]), e1 = __builtin_amdgcn_exp2f(a[1]), e2 = __builtin_amdgcn_exp2f(a[2]), e3 = __builtin_amdgcn_exp2f(a[3]);
      float e4 = __builtin_amdgcn_exp2f(bq[0]), e5 = __builtin_amdgcn_exp2f(bq[1]), e6 = __builtin_amdgcn_exp2f(bq[2]), e7 = __builtin_amdgcn_exp2f(bq[3]);
      u32x4 f = {cvtpk(e0, e1), cvtpk(e2, e3), cvtpk(e4, e5), cvtpk(e6, e7)};
      P[T][e] = f;
      lsum[e] = mfma(ones, __builtin_bit_cast(bf16x8, f), lsum[e]);
      PIN(P[T][e]); PIN(lsum[e]);
    };
    auto exp9 = [&](f32x4 s, unsigned dst) {
      const float e0 = __builtin_amdgcn_exp2f(s[0]), e1 = __builtin_amdgcn_exp2f(s[1]), e2 = __builtin_amdgcn_exp2f(s[2]), e3 = __builtin_amdgcn_exp2f(s[3]);
      l9 += (e0 + e1) + (e2 + e3);
      u32x2 v = {cvtpk(e0, e1), cvtpk(e2, e3)};
      *(LAS u32x2*)(size_t)dst = v; };
    auto setref = [&]() {
#pragma unroll
      for (int e = 0; e < 2; ++e) {
        f32x4 (&Sp)[2][2] = S[0];
        float d = fmaxf(fmaxf(fmaxf(Sp[0][e][0], Sp[0][e][1]), fmaxf(Sp[0][e][2], Sp[0][e][3])), fmaxf(fmaxf(Sp[1][e][0], Sp[1][e][1]), fmaxf(Sp[1][e][2], Sp[1][e][3])));
        d = rowmax(d);
        Sp[0][e] -= d; Sp[1][e] -= d; ncv[e] -= d;
      }
      SB();
    };
    auto kunit = [&](int un) {
      const int t = un >> 1, i = un & 1;
      if (un + 2 < 16) rdK(un + 2);
      SB();
      f32x4 a0 = ncv[0], a1 = ncv[1];
#pragma unroll
      for (int s = 0; s < 4; ++s) { a0 = mfma(kf[un % 3][s], qf[0][s], a0); a1 = mfma(kf[un % 3][s], qf[1][s], a1); }
      S[t & 1][i][0] = a0; S[t & 1][i][1] = a1;
      if (t > 0) expack(t - 1, i);
      if (un == 8 || un == 12) exp9(S9f, p9t + (un / 2 - 2) * 2048u);
      SB();
      if (dutymask & (1u << un)) {
        f32x4 acc9 = nc9;
#pragma unroll
        for (int s = 0; s < 4; ++s) acc9 = mfma(kf[un % 3][s], q9f[s], acc9);
        S9f = acc9;
      }
      SB();
      if (un == 3) {
        const float m = rowmax(fmaxf(fmaxf(S9f[0], S9f[1]), fmaxf(S9f[2], S9f[3])));
        if (G == 0) *(LAS float*)(size_t)(XCH + w * 64u + c * 4u) = m;
        SB();
      }
      if (un == 1) setref();
    };
    lds_bar();
    {
      const unsigned qb = lb + 4u * SLOT + c * 256u;
#pragma unroll
      for (int s = 0; s < 4; ++s) {
        const unsigned o = (((4u * s + G) ^ c) << 4);
        qf[0][s] = lds128(qb + (2u * qg + hf) * 4096u + o);
        qf[1][s] = lds128(qb + (2u * qg + (1u - hf)) * 4096u + o);
        q9f[s] = lds128(lb + MISC + c * 256u + o);
      }
    }
    rdK(0); rdK(1);
    KWAIT(cl63(cum(8) + 1 - posK(2)), 2); writeK(2); SB();
    kunit(0); kunit(1); issueV(0); issueV(1); issueV(2);
    lds_bar(); KWAIT(cl63(cum(8) + 7 - posK(3)), 3); writeK(3); kunit(2); kunit(3); issueV(3); issueV(4);
    lds_bar(); KWAIT(cl63(cum(8) + 11 - posK(4)), 4); writeK(4);
    {
      float m = -3.0e38f;
#pragma unroll
      for (int i = 0; i < 8; ++i) m = fmaxf(m, *(const LAS float*)(size_t)(XCH + i * 64u + c * 4u));
      nc9 = (f32x4){-m, -m, -m, -m};
      exp9(S9f + nc9, p9t);
      SB();
    }
    kunit(4); kunit(5);
    lds_bar4(); KWAIT(cl63(cum(8) + 11 - posK(5)), 5); writeK(5); kunit(6); kunit(7);
    lds_bar4(); KWAIT(cl63(cum(8) + 11 - posK(6)), 6); writeK(6); kunit(8); kunit(9);
    lds_bar4(); KWAIT(cl63(cum(8) + 11 - posK(7)), 7); writeK(7); kunit(10); kunit(11);
    lds_bar4(); kunit(12); kunit(13); issueV(5);
    wait_bar<63>(); kunit(14); kunit(15); issueV(6);
    expack(7, 0); expack(7, 1); exp9(S9f, p9t + 6 * 2048u);
    l9 += __shfl_xor(l9, 16); l9 += __shfl_xor(l9, 32);
    if (G == 0) *(LAS float*)(size_t)(XCH + 512u + w * 64u + c * 4u) = l9;
    {
      const bool bad = __any(!(lsum[0][0] <= THRP) || !(lsum[1][0] <= THRP) || !(l9 <= THRP));
      if (l == 0) *(LAS unsigned*)(size_t)(XCH + 1024u + w * 4u) = bad ? 1u : 0u; }
    if (G == 0) { const f32x4 st = {ncv[0][0], lsum[0][0], ncv[1][0], lsum[1][0]}; *(LAS f32x4*)(size_t)(lb + SLOT + 16384u + w * 256u + c * 16u) = st; }
    asm volatile("s_waitcnt vmcnt(12) lgkmcnt(0)\n\ts_barrier" : "+v"(finit) :: "memory");
    issueV(7);
    mst = (f32x4){ncv[0][0], lsum[0][0], ncv[1][0], lsum[1][0]};
    {
      const unsigned fl = *(const LAS unsigned*)(size_t)(XCH + 1024u + (l & 7u) * 4u);
      slow = rfl(__any(fl != 0u) ? 1u : 0u);
      if (__builtin_expect(slow != 0u, 0)) {
        const char* Kg = (const char*)(kc + (size_t)(b * NH + h) * SK * DH); const unsigned short* Vg = vc + (size_t)(b * NH + h) * SK * DH;
#pragma nounroll
        for (unsigned rep = 0; rep < (w == 0 ? 2u : 1u); ++rep)
          slow_tile_impl(rep ? q9f[0] : qf[0][0], rep ? q9f[1] : qf[0][1], rep ? q9f[2] : qf[0][2], rep ? q9f[3] : qf[0][3], Kg, Vg, srdOX, j * 36864u + (rep ? 8u : wv) * 4096u, l);
        asm volatile("s_waitcnt vmcnt(0) lgkmcnt(0)\n\ts_barrier" ::: "memory");
#pragma nounroll
        for (unsigned rep = 0; rep < (w == 0 ? 2u : 1u); ++rep)
#pragma unroll
          for (int s = 0; s < 4; ++s) { const unsigned o = (rep ? 8u : wv) * 4096u + s * 1024u + l * 16u;
            *(LAS u32x4*)(size_t)(lb + 2u * SLOT + o) = __builtin_amdgcn_raw_buffer_load_b128(srdOX, j * 36864u + o, 0, 16); }
        issueWO(0); issueWO(1); issueWO(2); issueWO(3);
        asm volatile("s_waitcnt vmcnt(0)\n\ts_barrier" ::: "memory");
      }
    }
  }
  SB();

  if (slow == 0u) {
  f32x4 O[2][8], O9[2] = {zero4, zero4};
#pragma unroll
  for (int u = 0; u < 8; ++u) { O[0][u] = zero4; O[1][u] = zero4; }
  {
    constexpr int PD = 6;
    const unsigned avb = (32u * hf + 4u * G + q4) * 256u + ((wv ^ (4u * (G & 1u) + q4)) << 5) + 8u * p;
    bf16x8 vf[8], p9[2];
    unsigned vb[2][8];
#pragma unroll
    for (int hh = 0; hh < 2; ++hh)
#pragma unroll
      for (int u = 0; u < 8; ++u) vb[hh][u] = opq(lb + (avb ^ (u << 5)) + hh * 4 * SLOT);
    const unsigned p9b = opq(lb + MISC + hf * 1024u + l * 16u);
    auto rdV = [&](int f) { const int t = f >> 3, u = f & 7, sl = VS[t];
      const unsigned a = vb[sl >> 2][u] + (sl & 3) * SLOT;
      vf[f & 7] = cat(ldstr(a), ldstr(a + 4096));
      if (u == 0) p9[t & 1] = lds128(p9b + t * 2048u); };
    auto vfr = [&](int f) { const int t = f >> 3, u = f & 7;
      O[0][u] = mfma(vf[f & 7], __builtin_bit_cast(bf16x8, P[t][0]), O[0][u]);
      O[1][u] = mfma(vf[f & 7], __builtin_bit_cast(bf16x8, P[t][1]), O[1][u]);
      if (u < 2) { O9[u] = mfma(vf[f & 7], p9[t & 1], O9[u]); PIN(O9[u]); }
      if (f + PD < 64) rdV(f + PD);
      SB(); };
#pragma unroll
    for (int f = 0; f < PD; ++f) rdV(f);
    SB();
#define VT(t, N) vfr(8 * t); vfr(8 * t + 1); wait_bar<N>(); vfr(8 * t + 2); vfr(8 * t + 3); vfr(8 * t + 4); vfr(8 * t + 5); vfr(8 * t + 6); vfr(8 * t + 7);
    VT(0, 12)
    {
      const f32x4 st = *(const LAS f32x4*)(size_t)(lb + SLOT + 16384u + (w ^ 4u) * 256u + c * 16u);
#pragma unroll
      for (int e = 0; e < 2; ++e) {
        const float na = mst[2 * e], nb = st[2 * (1 - e)], nm = fminf(na, nb);
        const float fa = __builtin_amdgcn_exp2f(nm - na), fb = __builtin_amdgcn_exp2f(nm - nb);
        fsc[e] = fa * __builtin_amdgcn_rcpf(mst[2 * e + 1] * fa + st[2 * (1 - e) + 1] * fb);
      }
      float l9t = 0.f;
#pragma unroll
      for (int i = 0; i < 8; ++i) l9t += *(const LAS float*)(size_t)(XCH + 512u + i * 64u + c * 4u);
      inv9 = __builtin_amdgcn_rcpf(l9t);
      PIN(fsc[0]); PIN(fsc[1]); PIN(inv9);
    }
    VT(1, 10) issueWO(2); VT(2, 11) issueWO(0); VT(3, 12) issueWO(1); VT(4, 13) issueWO(3); VT(5, 14) VT(6, 12)
#undef VT
#pragma unroll
    for (int f = 56; f < 64; ++f) vfr(f);
  }

  {
    const unsigned mine = lb + 5u * SLOT + w * 4608u;
#pragma unroll
    for (int u = 0; u < 8; ++u) { const f32x4 o = O[1][u] * fsc[1]; const u32x2 v = {cvtpk(o[0], o[1]), cvtpk(o[2], o[3])}; *(LAS u32x2*)(size_t)(mine + ((u ^ wv) << 9) + l * 8u) = v; }
    { const f32x4 o = O9[1] * inv9; const u32x2 v = {cvtpk(o[0], o[1]), cvtpk(o[2], o[3])}; *(LAS u32x2*)(size_t)(mine + 4096u + l * 8u) = v; }
  }
  asm volatile("s_waitcnt vmcnt(0) lgkmcnt(0)\n\ts_barrier" ::: "memory");
  {
    const unsigned oth = lb + 5u * SLOT + (w ^ 4u) * 4608u;
    auto up = [&](u32x2 v) { return (f32x4){__uint_as_float(v[0] << 16), __uint_as_float(v[0] & 0xffff0000u), __uint_as_float(v[1] << 16), __uint_as_float(v[1] & 0xffff0000u)}; };
#pragma unroll
    for (int u = 0; u < 8; ++u) O[0][u] = O[0][u] * fsc[0] + up(*(const LAS u32x2*)(size_t)(oth + ((u ^ wv) << 9) + l * 8u));
    O9[0] = O9[0] * inv9 + up(*(const LAS u32x2*)(size_t)(oth + 4096u + l * 8u));
  }
  const bool odd = (wv & 1u) != 0;
#pragma unroll
  for (int s = 0; s < 4; ++s) {
    unsigned a0 = cvtpk(O[0][2 * s][0], O[0][2 * s][1]), a1 = cvtpk(O[0][2 * s][2], O[0][2 * s][3]);
    unsigned b0 = cvtpk(O[0][2 * s + 1][0], O[0][2 * s + 1][1]), b1 = cvtpk(O[0][2 * s + 1][2], O[0][2 * s + 1][3]);
    const u32x4 f = odd ? (u32x4){b0, b1, a0, a1} : (u32x4){a0, a1, b0, b1};
    const unsigned o = wv * 4096u + ((s ^ (wv >> 1)) << 10) + l * 16u;
    __builtin_amdgcn_raw_buffer_store_b128(f, srdOX, j * 36864u + o, 0, 16);
    *(LAS u32x4*)(size_t)(lb + 2u * SLOT + o) = f;
  }
  { const u32x2 v = {cvtpk(O9[0][0], O9[0][1]), cvtpk(O9[0][2], O9[0][3])};
    const unsigned o = 8u * 4096u + ((wv >> 1) << 10) + l * 16u + (wv & 1u) * 8u;
    __builtin_amdgcn_raw_buffer_store_b64(v, srdOX, j * 36864u + o, 0, 16);
    *(LAS u32x2*)(size_t)(lb + 2u * SLOT + o) = v; }
  }

  unsigned hb = 1u;
  auto poll = [&](int stage) {
    const unsigned long long self1 = (1ull << j) | (1ull << (4u + j)), self2 = (1ull << (8u + g)) | (1ull << (16u + g));
    for (unsigned it = 0; it < 400000u; ++it) {
      unsigned v;
      asm volatile("buffer_load_dword %0, %1, %2, 0 offen sc1\n\ts_waitcnt vmcnt(0)" : "=v"(v) : "v"(fadr), "s"(srdF) : "memory");
      const unsigned long long ch = __ballot(v != finit) | self1 | self2;
      const bool ack1 = (ch & 0xF0ull) == 0xF0ull, ack2 = (ch & 0xFF0000ull) == 0xFF0000ull;
      if (stage == 1 && (ch & 0xFull) == 0xFull) break;
      if (stage == 2 && (ch & 0xFF00ull) == 0xFF00ull) break;
      if (stage == 3 && ack1 && ack2) break;
      if ((it & 63u) == 63u) {
        ++hb;
        if ((l == j && !ack1) || (stage >= 2 && l == 8u + g && !ack2)) __builtin_amdgcn_raw_buffer_store_b32(finit + hb, srdF, fadr, 0, 16);
      }
      __builtin_amdgcn_s_sleep(1);
    }
  };
  asm volatile("s_waitcnt vmcnt(0) lgkmcnt(0)\n\ts_barrier" ::: "memory");

  if (w == 0) { if (l == j) __builtin_amdgcn_raw_buffer_store_b32(finit + 1u, srdF, fadr, 0, 16); }
  else if (w <= 6u) { issueWO6(4); issueWO6(5); }
  {
    f32x4 acc[2][4], acc8 = zero4, acc9 = zero4, acc99 = zero4;
#pragma unroll
    for (int u = 0; u < 4; ++u) { acc[0][u] = zero4; acc[1][u] = zero4; }
    const unsigned aob = lb + (4u * G + q4) * 288u + 8u * p;
    unsigned ab[2][5];
#pragma unroll
    for (int hh = 0; hh < 2; ++hh) {
#pragma unroll
      for (int u = 0; u < 4; ++u) ab[hh][u] = opq(aob + (4u * hf + (u ^ qg)) * 32u + hh * 4u * SLOT);
      ab[hh][4] = opq(aob + 256u + hh * 4u * SLOT);
    }
    bf16x8 ar[8], bq[2][16];
    auto pk = [&](f32x4 a, f32x4 bb) { return (u32x4){cvtpk(a[0], a[1]), cvtpk(a[2], a[3]), cvtpk(bb[0], bb[1]), cvtpk(bb[2], bb[3])}; };
    auto rdI = [&](int it) {
      const int hd = it / 20, idx = it % 20, s = hd < 3 ? idx / 5 : idx % 4, u = hd < 3 ? idx % 5 : idx / 4, sl = WS[2 * hd + (s >> 1)];
      const unsigned a = ab[sl >> 2][u] + (sl & 3) * SLOT + (s & 1) * 32u * 288u;
      ar[it & 7] = cat(ldstr(a), ldstr(a + 16u * 288u)); };
    auto doI = [&](int it, int stop) {
      const int hd = it / 20, idx = it % 20, s = hd < 3 ? idx / 5 : idx % 4, u = hd < 3 ? idx % 5 : idx / 4;
      const bf16x8* B = bq[hd & 1];
      if (u < 4) {
        acc[0][u] = mfma(ar[it & 7], B[s], acc[0][u]); acc[1][u] = mfma(ar[it & 7], B[4 + s], acc[1][u]);
        if (u == 0) acc9 = mfma(ar[it & 7], B[8 + s], acc9);
        if (it + 6 < stop) rdI(it + 6);
        if (hd == 3 && s == 1 && u > 0) __builtin_amdgcn_raw_buffer_store_b128(pk(acc[0][u - 1], acc[1][u - 1]), srdPX, g * PXB + (qg * 9u + 4u * hf + ((u - 1) ^ qg)) * 1024u + l * 16u, 0, 16);
        if (hd == 3 && s == 2 && u == 1) __builtin_amdgcn_raw_buffer_store_b128(pk(acc9, acc9), srdPX, g * PXB + (36u + 4u * hf + qg) * 1024u + l * 16u, 0, 16);
        SB();
      } else {
        acc8 = mfma(ar[it & 7], B[12 + s], acc8);
        acc99 = mfma(ar[it & 7], B[8 + s], acc99);
        if (it + 6 < stop) rdI(it + 6);
        if (hd == 3 && s == 1) __builtin_amdgcn_raw_buffer_store_b128(pk(acc[0][3], acc[1][3]), srdPX, g * PXB + (qg * 9u + 4u * hf + (3u ^ qg)) * 1024u + l * 16u, 0, 16);
        SB();
      } };
    auto rdB = [&](int hd) {
      const unsigned ib = lb + (hd == 2 ? 0u : 2u * SLOT) + l * 16u;
#pragma unroll
      for (int i = 0; i < 8; ++i) bq[hd & 1][i] = lds128(ib + qg * 8192u + i * 1024u);
#pragma unroll
      for (int i = 0; i < 4; ++i) { bq[hd & 1][8 + i] = lds128(ib + 8u * 4096u + i * 1024u); bq[hd & 1][12 + i] = lds128(ib + wv * 4096u + i * 1024u); } };
#define D5(i, st) doI(i, st); doI(i + 1, st); doI(i + 2, st); doI(i + 3, st); doI(i + 4, st);
#define D20(i, st) D5(i, st) D5(i + 5, st) D5(i + 10, st) D5(i + 15, st)
    rdB(0);
#pragma unroll
    for (int it = 0; it < 6; ++it) rdI(it);
    SB();
    D20(0, 26)
    if (w == 0) {
      poll(1);
      if (l >= 24u && l < 28u) __builtin_amdgcn_raw_buffer_store_b32(finit + 1u, srdF, fadr, 0, 16);
    }
    asm volatile("s_waitcnt vmcnt(0)\n\ts_barrier" ::: "memory");

    {
      u32x4 xo[3][4]; u32x2 x9[3];
#pragma unroll
      for (int hd = 1; hd < 4; ++hd) {
        const unsigned so = ((j + hd) & 3u) * 36864u;
#pragma unroll
        for (int i = 0; i < 4; ++i) xo[hd - 1][i] = __builtin_amdgcn_raw_buffer_load_b128(srdOX, w * 4096u + l * 16u + i * 1024u, so, 16);
        x9[hd - 1] = __builtin_amdgcn_raw_buffer_load_b64(srdOX, 8u * 4096u + w * 512u + l * 8u, so, 16);
      }
      auto wrI = [&](int hd) { const unsigned ib = lb + (hd == 2 ? 0u : 2u * SLOT);
#pragma unroll
        for (int i = 0; i < 4; ++i) *(LAS u32x4*)(size_t)(ib + w * 4096u + l * 16u + i * 1024u) = xo[hd - 1][i];
        *(LAS u32x2*)(size_t)(ib + 8u * 4096u + w * 512u + l * 8u) = x9[hd - 1]; };
      wrI(1); wrI(2);
      lds_bar();

      rdB(1); SB();
      D5(20, 46) D5(25, 46) D5(30, 46)
      rdB(2); SB();
      D5(35, 46)
      lds_bar();
      if (w >= 1u && w <= 6u) { issueWO6(6); issueWO6(7); }
      wrI(3); SB();
      D20(40, 60)
      asm volatile("s_waitcnt vmcnt(0) lgkmcnt(0)\n\ts_barrier" ::: "memory");
      rdB(3);
#pragma unroll
      for (int it = 60; it < 66; ++it) rdI(it);
      SB();
      D20(60, 80)
    }
#undef D20
#undef D5
    { const u32x2 v = {cvtpk(acc8[0], acc8[1]), cvtpk(acc8[2], acc8[3])};
      __builtin_amdgcn_raw_buffer_store_b64(v, srdPX, g * PXB + (qg * 9u + 8u) * 1024u + l * 16u + hf * 8u, 0, 16); }
    if (w == 0u) __builtin_amdgcn_raw_buffer_store_b128(pk(acc99, acc99), srdPX, g * PXB + 44u * 1024u + l * 16u, 0, 16);
  }

  asm volatile("s_waitcnt vmcnt(0)\n\ts_barrier" ::: "memory");

  if (w == 0) {
    if (l == 8u + g) __builtin_amdgcn_raw_buffer_store_b32(finit + 1u, srdF, fadr, 0, 16);
    poll(2);
    if (l >= 32u && l < 40u) __builtin_amdgcn_raw_buffer_store_b32(finit + 1u, srdF, fadr, 0, 16);
  }
  asm volatile("s_barrier" ::: "memory");

  {
    const unsigned un = g + 8u * (w - 1u);
    if (w >= 1u && un < 45u) {
      u32x4 pv[8];
#pragma unroll
      for (int gg = 0; gg < 8; ++gg) pv[gg] = __builtin_amdgcn_raw_buffer_load_b128(srdPX, un * 1024u + l * 16u, gg * PXB, 16);
      float sm[8];
#pragma unroll
      for (int k = 0; k < 4; ++k) {
        float lo = 0.f, hi = 0.f;
#pragma unroll
        for (int gg = 0; gg < 8; ++gg) { lo += __uint_as_float(pv[gg][k] << 16); hi += __uint_as_float(pv[gg][k] & 0xffff0000u); }
        sm[2 * k] = lo; sm[2 * k + 1] = hi;
      }
      const unsigned qa = un < 36u ? 2u * (un / 9u) : 8u, nt = un < 36u ? un % 9u : un - 36u;
      const u32x2 va = {cvtpk(sm[0], sm[1]), cvtpk(sm[2], sm[3])}, vb = {cvtpk(sm[4], sm[5]), cvtpk(sm[6], sm[7])};
      unsigned short* op = out + (size_t)(b * SQ + qa * 16u + c) * DM + j * 144u + nt * 16u + G * 4u;
      *(u32x2*)op = va;
      if (un < 36u) *(u32x2*)(op + 16 * DM) = vb;
    }
  }

  if (w == 0) poll(3);
}

extern "C" void kernel_launch(void* const* d_in, const int* in_sizes, int n_in, void* d_out, int out_size, void* d_ws, size_t ws_size, hipStream_t stream) {
  const unsigned short* x = (const unsigned short*)d_in[0];
  const unsigned short* kc = (const unsigned short*)d_in[1];
  const unsigned short* vc = (const unsigned short*)d_in[2];
  const unsigned short* wq = (const unsigned short*)d_in[3];
  const unsigned short* wo = (const unsigned short*)d_in[4];
  (void)hipFuncSetAttribute((const void*)k1_attn, hipFuncAttributeMaxDynamicSharedMemorySize, LDS_BYTES);
  k1_attn<<<256, 512, LDS_BYTES, stream>>>(x, kc, vc, wq, wo, (unsigned short*)d_out, (char*)d_ws);
}
```
